```python
import torch

def _softmax(x, axis=-1):
    return torch.softmax(x, dim=axis)
def _gelu(x):
    return torch.nn.functional.gelu(x, approximate="tanh")
def _silu(x):
    return torch.nn.functional.silu(x)
def _sigmoid(x):
    return torch.sigmoid(x)
def _layernorm(x, axis=-1, eps=1e-5):
    m = x.mean(dim=axis, keepdim=True)
    v = x.var(dim=axis, keepdim=True, unbiased=False)
    return (x - m) / torch.sqrt(v + eps)
def _scatter_add(tgt, idx, src, axis=0):
    return tgt.clone().index_add_(axis, idx, src)
def _segment_sum(src, seg, n):
    out = torch.zeros((n,) + src.shape[1:], dtype=src.dtype)
    return out.index_add_(0, seg, src)
def _pad(x, pw):
    flat = tuple(v for pair in reversed(pw) for v in pair)
    return torch.nn.functional.pad(x, flat)

kernel_name = 'parallel_sum__sliding_block__glu_gated_geglu__prompt'

def setup_inputs():

    _w = (lambda a: (lambda t: t.to(torch.bfloat16) if t.is_floating_point() else t)(torch.as_tensor(a)))
    def _A_setup():
        B, S, D = 1, 2048, 512
        return {
            "x": _w((lambda *s: torch.randn(*s, dtype=torch.bfloat16) * 0.05)(B, S, D)),
            "wqkv": _w((lambda *s: torch.randn(*s, dtype=torch.bfloat16) * 0.05)(D, 3 * D)),
            "wo": _w((lambda *s: torch.randn(*s, dtype=torch.bfloat16) * 0.05)(D, D)),
        }
    _A_in = _A_setup()

    def _B_setup():
        B, S, Dm, Df = 1, 2048, 512, 1792
        return {
            "x": _w((lambda *s: torch.randn(*s, dtype=torch.bfloat16) * 0.05)(B, S, Dm)),
            "w_gate": _w((lambda *s: torch.randn(*s, dtype=torch.bfloat16) * 0.8)(Dm, Df) * (Dm ** -0.5)),
            "w_up": _w((lambda *s: torch.randn(*s, dtype=torch.bfloat16) * 0.8)(Dm, Df) * (Dm ** -0.5)),
            "w_down": _w((lambda *s: torch.randn(*s, dtype=torch.bfloat16) * 0.8)(Df, Dm) * (Df ** -0.5)),
        }
    _B_in = _B_setup()

    return {
        "A_x": _A_in["x"],
        "A_wqkv": _A_in["wqkv"],
        "A_wo": _A_in["wo"],
        "B_x": _B_in["x"],
        "B_w_gate": _B_in["w_gate"],
        "B_w_up": _B_in["w_up"],
        "B_w_down": _B_in["w_down"],
    }

def reference(A_x, A_wqkv, A_wo, B_x, B_w_gate, B_w_up, B_w_down):
    def _A_ref(x, wqkv, wo):
        B, S, D = x.shape
        H = 32; Dh = D // H; W = 256
        qkv = (lambda x, *d: x.permute(*d))((x @ wqkv).reshape(B, S, 3, H, Dh), 2, 0, 3, 1, 4)
        q, k, v = qkv[0], qkv[1], qkv[2]
        scores = torch.einsum("bhqd,bhkd->bhqk", q, k) * (Dh ** -0.5)
        idx = torch.arange(S)
        mask = (idx[:, None] - idx[None, :] >= 0) & (idx[:, None] - idx[None, :] < W)
        scores = torch.where(mask[None, None], scores, -1e9)
        p = _softmax(scores, axis=-1)
        o = torch.einsum("bhqk,bhkd->bhqd", p, v)
        return (lambda x, *d: x.permute(*d))(o, 0, 2, 1, 3).reshape(B, S, D) @ wo

    def _B_ref(x, w_gate, w_up, w_down):
        g = torch.einsum("bsd,df->bsf", x, w_gate)
        u = torch.einsum("bsd,df->bsf", x, w_up)
        h = _gelu(g) * u
        return torch.einsum("bsf,fd->bsd", h, w_down)

    x = A_x
    xn = _layernorm(x, axis=-1)
    return x + _A_ref(xn, A_wqkv, A_wo) + _B_ref(xn, B_w_gate, B_w_up, B_w_down)
```

```cpp
#include <hip/hip_runtime.h>
#include <hip/hip_bf16.h>
typedef __hip_bfloat16 bf16;
#define S_ 2048
#define D_ 512
#define F_ 1792
__device__ __forceinline__ float bf2f(bf16 v){ return __bfloat162float(v);}
__device__ __forceinline__ float rbf(float v){ return __bfloat162float(__float2bfloat16(v)); }

__global__ void ln_kernel(const bf16* x, bf16* xn){
  int row = blockIdx.x; int t = threadIdx.x;
  float v[8]; float s=0;
  for(int i=0;i<8;i++){ v[i]=bf2f(x[row*D_+t*8+i]); s+=v[i]; }
  for(int o=32;o>0;o>>=1) s+=__shfl_xor(s,o);
  float m=s/D_; float q=0;
  for(int i=0;i<8;i++){ float d=v[i]-m; q+=d*d; }
  for(int o=32;o>0;o>>=1) q+=__shfl_xor(q,o);
  float var=q/D_;
  float r=rsqrtf(var+1e-5f);
  for(int i=0;i<8;i++) xn[row*D_+t*8+i]=__float2bfloat16((v[i]-m)*r);
}
__global__ void gemm_naive(const bf16* A, int lda, const bf16* B, int ldb, float* C, int ldc, int K, int accumulate){
  int n = blockIdx.x*64+threadIdx.x; int m = blockIdx.y*4+threadIdx.y;
  float acc=0;
  for(int k=0;k<K;k++) acc += bf2f(A[m*lda+k])*bf2f(B[k*ldb+n]);
  if(accumulate) C[m*ldc+n]+=acc; else C[m*ldc+n]=acc;
}
__global__ void glu_kernel(const float* g, const float* u, bf16* h){
  int i = blockIdx.x*256+threadIdx.x;
  float x=g[i];
  float t=tanhf(0.7978845608028654f*(x+0.044715f*x*x*x));
  h[i]=__float2bfloat16(0.5f*x*(1.f+t)*u[i]);
}
__global__ void attn_kernel(const float* qkv, bf16* o){
  int q = blockIdx.x; int h = threadIdx.x;
  float qv[16]; for(int d=0;d<16;d++) qv[d]=rbf(qkv[q*1536+h*16+d]);
  int k0 = q-255<0?0:q-255;
  float m=-1e30f;
  for(int k=k0;k<=q;k++){ float s=0; for(int d=0;d<16;d++) s+=qv[d]*rbf(qkv[k*1536+512+h*16+d]); s*=0.25f; m=fmaxf(m,s);}
  float l=0; float acc[16]; for(int d=0;d<16;d++) acc[d]=0;
  for(int k=k0;k<=q;k++){ float s=0; for(int d=0;d<16;d++) s+=qv[d]*rbf(qkv[k*1536+512+h*16+d]); s*=0.25f; float p=__expf(s-m); l+=p; for(int d=0;d<16;d++) acc[d]+=p*rbf(qkv[k*1536+1024+h*16+d]); }
  for(int d=0;d<16;d++) o[q*512+h*16+d]=__float2bfloat16(acc[d]/l);
}
__global__ void final_kernel(const bf16* x, const float* acc, bf16* out){
  int i = blockIdx.x*256+threadIdx.x;
  out[i]=__float2bfloat16(bf2f(x[i])+acc[i]);
}
extern "C" void kernel_launch(void* const* d_in, const int* in_sizes, int n_in,
                              void* d_out, int out_size, void* d_ws, size_t ws_size,
                              hipStream_t stream) {
  const bf16* x=(const bf16*)d_in[0]; const bf16* wqkv=(const bf16*)d_in[1]; const bf16* wo=(const bf16*)d_in[2];
  const bf16* wg=(const bf16*)d_in[4]; const bf16* wu=(const bf16*)d_in[5]; const bf16* wd=(const bf16*)d_in[6];
  char* ws=(char*)d_ws;
  bf16* xn=(bf16*)ws; ws+=S_*D_*2;
  float* qkv=(float*)ws; ws+=S_*1536*4;
  float* g=(float*)ws; ws+=S_*F_*4;
  float* u=(float*)ws; ws+=S_*F_*4;
  bf16* h=(bf16*)ws; ws+=S_*F_*2;
  bf16* o=(bf16*)ws; ws+=S_*D_*2;
  float* acc=(float*)ws; ws+=S_*D_*4;
  ln_kernel<<<S_,64,0,stream>>>(x,xn);
  gemm_naive<<<dim3(1536/64,S_/4),dim3(64,4),0,stream>>>(xn,D_,wqkv,1536,qkv,1536,D_,0);
  gemm_naive<<<dim3(F_/64,S_/4),dim3(64,4),0,stream>>>(xn,D_,wg,F_,g,F_,D_,0);
  gemm_naive<<<dim3(F_/64,S_/4),dim3(64,4),0,stream>>>(xn,D_,wu,F_,u,F_,D_,0);
  glu_kernel<<<S_*F_/256,256,0,stream>>>(g,u,h);
  attn_kernel<<<S_,32,0,stream>>>(qkv,o);
  gemm_naive<<<dim3(D_/64,S_/4),dim3(64,4),0,stream>>>(o,D_,wo,D_,acc,D_,D_,0);
  gemm_naive<<<dim3(D_/64,S_/4),dim3(64,4),0,stream>>>(h,F_,wd,D_,acc,D_,F_,1);
  final_kernel<<<S_*D_/256,256,0,stream>>>(x,acc,(bf16*)d_out);
}
```

```cpp
#include <hip/hip_runtime.h>
#include <hip/hip_bf16.h>
#include <stdint.h>

typedef short bf16x8 __attribute__((ext_vector_type(8)));
typedef short s16x4 __attribute__((ext_vector_type(4)));
typedef float f32x2 __attribute__((ext_vector_type(2)));
typedef float f32x4 __attribute__((ext_vector_type(4)));
typedef float f32x16 __attribute__((ext_vector_type(16)));
typedef unsigned u32x4 __attribute__((ext_vector_type(4)));
typedef unsigned u32x2 __attribute__((ext_vector_type(2)));
typedef __bf16 bf16x2_t __attribute__((ext_vector_type(2)));
#define LAS __attribute__((address_space(3)))
#define GAS __attribute__((address_space(1)))
#define DI __device__ __forceinline__

constexpr int SEQ = 2048, DM = 512, FF = 1792, NH = 32;
constexpr int KSTEPS2 = 144;
constexpr size_t WS_QKV = 0;
constexpr size_t WS_A2  = 6291456;

DI unsigned cvt_pk_bf16(float lo, float hi) {
    f32x2 v = {lo, hi};
    bf16x2_t r = __builtin_convertvector(v, bf16x2_t);
    return __builtin_bit_cast(unsigned, r);
}
DI float dot2_bf16(unsigned a, unsigned b, float c) {
    return __builtin_amdgcn_fdot2_f32_bf16(__builtin_bit_cast(bf16x2_t, a), __builtin_bit_cast(bf16x2_t, b), c, false);
}
DI void glds16(const void* sbase, unsigned voff, unsigned ldsaddr) {
    asm volatile("s_mov_b32 m0, %2\n\ts_nop 0\n\tglobal_load_lds_dwordx4 %0, %1"
                 :: "v"(voff), "s"(sbase), "s"(ldsaddr) : "memory");
}
DI bf16x8 lds_b128(unsigned addr) { return *(const LAS bf16x8*)(uintptr_t)addr; }
DI void lds_st128(unsigned addr, u32x4 v) { *(LAS u32x4*)(uintptr_t)addr = v; }
DI s16x4 lds_tr(unsigned addr) { return __builtin_amdgcn_ds_read_tr16_b64_v4i16((LAS s16x4*)(uintptr_t)addr); }
DI bf16x8 cat8(s16x4 a, s16x4 b) { return __builtin_shufflevector(a, b, 0, 1, 2, 3, 4, 5, 6, 7); }
#define WAITV_BAR(N) asm volatile("s_waitcnt vmcnt(" #N ") lgkmcnt(0)\n\ts_barrier" ::: "memory")
#define MFMA16(a, b, c) __builtin_amdgcn_mfma_f32_16x16x32_bf16(a, b, c, 0, 0, 0)
#define MFMA32(a, b, c) __builtin_amdgcn_mfma_f32_32x32x16_bf16(a, b, c, 0, 0, 0)
#define SBAR() __builtin_amdgcn_sched_barrier(0)

#define TS(i)
#define TSDECL
#define TSOUT(kid,n)

constexpr int G1_WSLOT = 40960, G1_XSLOT = 16384;
constexpr int G1_LDS_X = 0, G1_LDS_W = 2 * G1_XSLOT;
constexpr int G1_LDS_BYTES = G1_LDS_W + 3 * G1_WSLOT;

struct G1Ctx {
    const char* sb[5];
    unsigned v0, vF;
    unsigned step0, stepF;
    int wave;
};
template <int I> DI void g1_issue(G1Ctx& c) {
    constexpr unsigned wbase = G1_LDS_W + (I % 3) * G1_WSLOT;
#pragma unroll
    for (int u = 0; u < 5; ++u) {
        glds16(c.sb[u], u == 0 ? c.v0 : c.vF, wbase + u * 8192 + c.wave * 1024);
        c.sb[u] += (u == 0 ? c.step0 : c.stepF);
    }
}
struct G1Frag { bf16x8 x[4]; bf16x8 w[5]; };
struct G1Addr { unsigned xa[2]; unsigned wa[3]; unsigned xw; };

template <int S> DI void g1_read(G1Frag& f, const G1Addr& a) {
    constexpr int I = S >> 1, e = S & 1;
    constexpr unsigned wo = G1_LDS_W + (I % 3) * G1_WSLOT + e * 4096, xo = G1_LDS_X + (I & 1) * G1_XSLOT;
#pragma unroll
    for (int i = 0; i < 4; ++i) f.x[i] = lds_b128(a.xa[e] + xo + i * 2048);
    f.w[0] = cat8(lds_tr(a.wa[0] + wo), lds_tr(a.wa[0] + wo + 512));
    f.w[1] = cat8(lds_tr(a.wa[1] + wo + 8192), lds_tr(a.wa[1] + wo + 8192 + 512));
    f.w[2] = cat8(lds_tr(a.wa[2] + wo + 8192), lds_tr(a.wa[2] + wo + 8192 + 512));
    f.w[3] = cat8(lds_tr(a.wa[1] + wo + 24576), lds_tr(a.wa[1] + wo + 24576 + 512));
    f.w[4] = cat8(lds_tr(a.wa[2] + wo + 24576), lds_tr(a.wa[2] + wo + 24576 + 512));
}
DI void g1_mma(f32x4 (&acc)[5][4], const G1Frag& f) {
#pragma unroll
    for (int j = 0; j < 5; ++j)
#pragma unroll
        for (int i = 0; i < 4; ++i) acc[j][i] = MFMA16(f.w[j], f.x[i], acc[j][i]);
}
DI u32x4 g1_norm(u32x4 v, float rstd, float nmr) {
    u32x4 o;
#pragma unroll
    for (int r = 0; r < 4; ++r) {
        const float lo = __uint_as_float(v[r] << 16), hi = __uint_as_float(v[r] & 0xffff0000u);
        o[r] = cvt_pk_bf16(__builtin_fmaf(lo, rstd, nmr), __builtin_fmaf(hi, rstd, nmr));
    }
    return o;
}
template <int I> DI void g1_xwrite(const u32x4 (&xr)[2][8], const float (&rstd)[2], const float (&nmr)[2], const G1Addr& a) {
    constexpr unsigned xo = G1_LDS_X + (I & 1) * G1_XSLOT;
    lds_st128(a.xw + xo, g1_norm(xr[0][I], rstd[0], nmr[0]));
    lds_st128((a.xw ^ 64) + xo + 1024, g1_norm(xr[1][I], rstd[1], nmr[1]));
}
DI float dpp_sum8(float v) {
    v += __builtin_bit_cast(float, __builtin_amdgcn_update_dpp(0, __builtin_bit_cast(int, v), 0xB1, 0xf, 0xf, true));
    v += __builtin_bit_cast(float, __builtin_amdgcn_update_dpp(0, __builtin_bit_cast(int, v), 0x4E, 0xf, 0xf, true));
    v += __builtin_bit_cast(float, __builtin_amdgcn_update_dpp(0, __builtin_bit_cast(int, v), 0x141, 0xf, 0xf, true));
    return v;
}

template <int I> DI void g1_interval(G1Ctx& c, const G1Addr& a, f32x4 (&acc)[5][4], G1Frag& f0, G1Frag& f1,
                                     const u32x4 (&xr)[2][8], const float (&rstd)[2], const float (&nmr)[2]) {
    if constexpr (I + 2 < 8) g1_issue<I + 2>(c);
    g1_read<2 * I>(f0, a);
    if constexpr (I > 0) g1_mma(acc, f1);
    SBAR();
    g1_read<2 * I + 1>(f1, a);
    if constexpr (I + 1 < 8) g1_xwrite<I + 1>(xr, rstd, nmr, a);
    g1_mma(acc, f0);
    SBAR();
    if constexpr (I + 1 < 8) {
        if constexpr (I + 2 < 8) WAITV_BAR(5); else WAITV_BAR(0);
    }
}

__global__ __launch_bounds__(512, 2) void k_gemm1(const __hip_bfloat16* __restrict__ x_, const __hip_bfloat16* __restrict__ wqkv_,
        const __hip_bfloat16* __restrict__ wg_, const __hip_bfloat16* __restrict__ wu_, char* __restrict__ ws) {
    const int b = blockIdx.x, xcd = b & 7, idx = b >> 3;
    TSDECL TS(0);
    const int tm = (xcd >> 2) * 8 + (idx >> 2), tn = (xcd & 3) * 4 + (idx & 3);
    const int tid = threadIdx.x, lane = tid & 63;
    const int wave = __builtin_amdgcn_readfirstlane(tid >> 6);
    const int wr = wave >> 2, wc = wave & 3;
    const bool typeA = tn < 14;
    const char* wqkv = (const char*)wqkv_; const char* wg = (const char*)wg_; const char* wu = (const char*)wu_;
    const int qu0 = typeA ? tn : (tn == 14 ? 14 : 19);
    const unsigned strF = typeA ? 3584u : 3072u;
    G1Ctx c; c.wave = wave;
    {
        const int r = lane >> 3, ch = lane & 7;
        const int g = ((r >> 1) & 1) | ((wave & 1) << 1);
        const unsigned colw = (unsigned)((ch ^ (g << 1)) * 16);
        c.v0 = (8 * wave + r) * 3072u + colw; c.vF = (8 * wave + r) * strF + colw;
        c.step0 = 64 * 3072u; c.stepF = 64 * strF;
        c.sb[0] = wqkv + qu0 * 128;
        c.sb[1] = typeA ? wg + tn * 256 : wqkv + (qu0 + 1) * 128;
        c.sb[2] = c.sb[1] + 128;
        c.sb[3] = typeA ? wu + tn * 256 : wqkv + (qu0 + 3) * 128;
        c.sb[4] = c.sb[3] + 128;
    }
    g1_issue<0>(c);
    u32x4 xr[2][8];
    {
        const char* xp = (const char*)x_ + ((size_t)tm * 128 + 16 * wave + (lane >> 3)) * 1024 + (lane & 7) * 16;
#pragma unroll
        for (int s = 0; s < 8; ++s) { xr[0][s] = *(const u32x4*)(xp + s * 128); xr[1][s] = *(const u32x4*)(xp + 8192 + s * 128); }
    }
    G1Addr a;
    {
        const int t = lane & 15, q = lane >> 4;
        a.xa[0] = (unsigned)((64 * wr + t) * 128 + ((q ^ (t >> 1)) * 16));
        a.xa[1] = a.xa[0] ^ 64;
        const int r = (lane & 15) >> 2, p = lane & 3;
        const int g = ((r >> 1) & 1) | ((q & 1) << 1);
        const unsigned rowoff = (unsigned)((8 * q + r) * 128 + (p >> 1) * 16 + (p & 1) * 8);
        a.wa[0] = rowoff + ((wc ^ g) << 5);
        a.wa[1] = rowoff + (((2 * (wc & 1)) ^ g) << 5) + (wc >> 1) * 8192;
        a.wa[2] = a.wa[1] ^ 32;
        const int row = 16 * wave + (lane >> 3);
        a.xw = (unsigned)(row * 128 + (((lane & 7) ^ ((row >> 1) & 7)) * 16));
    }
    TS(1);
    float rstd[2], nmr[2];
    {
        const unsigned ones = 0x3f803f80u;
#pragma unroll
        for (int h = 0; h < 2; ++h) {
            float s1a = 0.f, s2a = 0.f, s1b = 0.f, s2b = 0.f;
#pragma unroll
            for (int s = 0; s < 8; ++s) {
                s1a = dot2_bf16(xr[h][s][0], ones, s1a); s2a = dot2_bf16(xr[h][s][0], xr[h][s][0], s2a);
                s1b = dot2_bf16(xr[h][s][1], ones, s1b); s2b = dot2_bf16(xr[h][s][1], xr[h][s][1], s2b);
                s1a = dot2_bf16(xr[h][s][2], ones, s1a); s2a = dot2_bf16(xr[h][s][2], xr[h][s][2], s2a);
                s1b = dot2_bf16(xr[h][s][3], ones, s1b); s2b = dot2_bf16(xr[h][s][3], xr[h][s][3], s2b);
            }
            const float s1 = dpp_sum8(s1a + s1b), s2 = dpp_sum8(s2a + s2b);
            const float mean = s1 * (1.f / 512.f);
            const float var = fmaxf(s2 * (1.f / 512.f) - mean * mean, 0.f) + 1e-5f;
            rstd[h] = __builtin_amdgcn_rsqf(var); nmr[h] = -mean * rstd[h];
        }
    }
    SBAR();
    TS(2);
    g1_issue<1>(c);
    g1_xwrite<0>(xr, rstd, nmr, a);
    f32x4 acc[5][4];
#pragma unroll
    for (int j = 0; j < 5; ++j)
#pragma unroll
        for (int i = 0; i < 4; ++i) acc[j][i] = f32x4{0.f, 0.f, 0.f, 0.f};
    G1Frag f0, f1;
    WAITV_BAR(5);
    TS(3);
    g1_interval<0>(c, a, acc, f0, f1, xr, rstd, nmr); TS(4);
    g1_interval<1>(c, a, acc, f0, f1, xr, rstd, nmr); TS(5);
    g1_interval<2>(c, a, acc, f0, f1, xr, rstd, nmr); TS(6);
    g1_interval<3>(c, a, acc, f0, f1, xr, rstd, nmr); TS(7);
    g1_interval<4>(c, a, acc, f0, f1, xr, rstd, nmr); TS(8);
    g1_interval<5>(c, a, acc, f0, f1, xr, rstd, nmr); TS(9);
    g1_interval<6>(c, a, acc, f0, f1, xr, rstd, nmr); TS(10);
    g1_interval<7>(c, a, acc, f0, f1, xr, rstd, nmr); TS(11);
    g1_mma(acc, f1);
    TS(12);
    const int t = lane & 15, q = lane >> 4;
    char* qkv = ws + WS_QKV; char* a2 = ws + WS_A2;
    constexpr float QSCALE = 0.25f * 1.4426950408889634f;
    constexpr float C1 = -2.f * 0.7978845608028654f * 1.4426950408889634f, C2 = C1 * 0.044715f;
#pragma unroll
    for (int i = 0; i < 4; ++i) {
        const int tok = tm * 128 + 64 * wr + 16 * i + t;
        auto store_qkv = [&](int j, int unit, int tile) {
            const int hcol = unit * 4 + tile;
            const float sc = hcol < 32 ? QSCALE : 1.f;
            u32x2 pk = {cvt_pk_bf16(acc[j][i][0] * sc, acc[j][i][1] * sc), cvt_pk_bf16(acc[j][i][2] * sc, acc[j][i][3] * sc)};
            *(u32x2*)(qkv + ((size_t)hcol * SEQ + tok) * 32 + q * 8) = pk;
        };
        store_qkv(0, qu0, wc);
        if (typeA) {
            float h[8];
#pragma unroll
            for (int p = 0; p < 2; ++p)
#pragma unroll
                for (int r = 0; r < 4; ++r) {
                    const float ga = acc[1 + p][i][r], ua = acc[3 + p][i][r];
                    const float z = ga * __builtin_fmaf(ga * ga, C2, C1);
                    const float e = __builtin_amdgcn_exp2f(z);
                    h[p * 4 + r] = ga * ua * __builtin_amdgcn_rcpf(1.f + e);
                }
            u32x4 pk = {cvt_pk_bf16(h[0], h[1]), cvt_pk_bf16(h[2], h[3]), cvt_pk_bf16(h[4], h[5]), cvt_pk_bf16(h[6], h[7])};
            const int kstep = 32 + 2 * (4 * tn + wc) + (q >> 1);
            *(u32x4*)(a2 + ((size_t)((tok >> 5) * KSTEPS2 + kstep) * 64 + (q & 1) * 32 + (tok & 31)) * 16) = pk;
        } else {
            store_qkv(1, qu0 + 1 + (wc >> 1), 2 * (wc & 1));
            store_qkv(2, qu0 + 1 + (wc >> 1), 2 * (wc & 1) + 1);
            store_qkv(3, qu0 + 3 + (wc >> 1), 2 * (wc & 1));
            store_qkv(4, qu0 + 3 + (wc >> 1), 2 * (wc & 1) + 1);
        }
    }
    TS(13);
    TSOUT(0, 14);
}

constexpr int AT_LDS_K = 0, AT_LDS_V = 16384, AT_LDS_BYTES = 16384 + 32768;

DI float exp2_clamp(float v) { return __builtin_amdgcn_fmed3f(__builtin_amdgcn_exp2f(v), 0.f, 1.f); }

__global__ __launch_bounds__(512, 2) void k_attn(char* __restrict__ ws) {
    const int b = blockIdx.x, xcd = b & 7, idx = b >> 3;
    TSDECL TS(0);
    const int head = xcd * 4 + (idx & 3), tg = idx >> 2;
    const int tid = threadIdx.x, lane = tid & 63;
    const int wave = __builtin_amdgcn_readfirstlane(tid >> 6);
    const char* Q = ws + WS_QKV + (size_t)head * SEQ * 32;
    const char* K = Q + (size_t)32 * SEQ * 32;
    const char* V = K + (size_t)32 * SEQ * 32;
    const int a = lane & 31, hh = lane >> 5;
    const int qt = 8 * tg + wave;
    {
        const bool lo_ok = tg > 0;
        const int kt0 = lo_ok ? qt - 8 : qt;
        const u32x4 k0 = *(const u32x4*)(K + kt0 * 1024 + a * 32 + hh * 16);
        const u32x4 k1 = *(const u32x4*)(K + qt * 1024 + a * 32 + hh * 16);
        u32x4 v0 = *(const u32x4*)(V + kt0 * 1024 + lane * 16);
        const u32x4 v1 = *(const u32x4*)(V + qt * 1024 + lane * 16);
        const unsigned one = (lane & 1) ? 0u : 0x3f80u;
        u32x4 c1 = {one, 0u, one, 0u}, c0 = c1;
        if (!lo_ok) { v0 = u32x4{0u, 0u, 0u, 0u}; c0 = v0; }
        const unsigned va = AT_LDS_V + wave * 2048 + (lane >> 1) * 64 + (lane & 1) * 16;
        lds_st128(va + 32, c0); lds_st128(va + 16384 + 32, c1);
        lds_st128(AT_LDS_K + wave * 1024 + lane * 16, k0); lds_st128(AT_LDS_K + 8192 + wave * 1024 + lane * 16, k1);
        lds_st128(va, v0); lds_st128(va + 16384, v1);
    }
    const bf16x8 qf = *(const bf16x8*)(Q + (size_t)(qt * 32 + a) * 32 + hh * 16);
    TS(1);
    __syncthreads();
    TS(2);
    const unsigned kaddr = AT_LDS_K + wave * 1024 + lane * 16;
    bf16x8 kf[9];
#pragma unroll
    for (int j = 0; j < 9; ++j) kf[j] = lds_b128(kaddr + j * 1024);
    const f32x16 zero = {};
    float m;
    {
        const f32x16 sf = MFMA32(kf[0], qf, zero);
        const f32x16 sd = MFMA32(kf[8], qf, zero);
        float mt[8];
#pragma unroll
        for (int j = 1; j < 8; ++j) {
            const f32x16 s = MFMA32(kf[j], qf, zero);
            float v = fmaxf(s[0], s[1]);
#pragma unroll
            for (int r = 2; r < 16; r += 2) v = fmaxf(fmaxf(v, s[r]), s[r + 1]);
            mt[j] = (qt - 8 + j >= 0) ? v : -INFINITY;
        }
        float md = -INFINITY, mf = -INFINITY;
#pragma unroll
        for (int r = 0; r < 16; ++r) {
            const int bb = (r & 3) + 8 * (r >> 2) + 4 * hh;
            md = fmaxf(md, bb <= a ? sd[r] : -INFINITY);
            mf = fmaxf(mf, bb <= a ? -INFINITY : sf[r]);
        }
        mt[0] = fmaxf(md, qt >= 8 ? mf : -INFINITY);
        m = fmaxf(fmaxf(fmaxf(mt[0], mt[1]), fmaxf(mt[2], mt[3])), fmaxf(fmaxf(mt[4], mt[5]), fmaxf(mt[6], mt[7])));
        const auto sw = __builtin_amdgcn_permlane32_swap(__float_as_uint(m), __float_as_uint(m), false, false);
        m = fmaxf(__uint_as_float(sw[0]), __uint_as_float(sw[1]));
    }
    TS(3);
    f32x16 negm;
#pragma unroll
    for (int r = 0; r < 16; ++r) negm[r] = -m;
    const unsigned vaddr = AT_LDS_V + wave * 2048 + (4 * hh + ((lane & 15) >> 2)) * 64 + ((lane >> 4) & 1) * 32 + (lane & 3) * 8;
    f32x16 o = zero;
    auto pv = [&](const u32x4& p0, const u32x4& p1, int j) {
        const unsigned va = vaddr + j * 2048;
        const bf16x8 v0 = cat8(lds_tr(va), lds_tr(va + 512));
        const bf16x8 v1 = cat8(lds_tr(va + 1024), lds_tr(va + 1536));
        o = MFMA32(v0, __builtin_bit_cast(bf16x8, p0), o);
        o = MFMA32(v1, __builtin_bit_cast(bf16x8, p1), o);
    };
    {
        const f32x16 sf = MFMA32(kf[0], qf, negm);
        const f32x16 sd = MFMA32(kf[8], qf, negm);
        unsigned pk[8], mk[8];
#pragma unroll
        for (int r = 0; r < 16; r += 2) {
            const int bb = (r & 3) + 8 * (r >> 2) + 4 * hh;
            const bool c0 = bb <= a, c1 = bb + 1 <= a;
            const float v0 = c0 ? sd[r] : sf[r];
            const float v1 = c1 ? sd[r + 1] : sf[r + 1];
            pk[r >> 1] = cvt_pk_bf16(exp2_clamp(v0), exp2_clamp(v1));
            mk[r >> 1] = (c0 ? 0xffffu : 0u) | (c1 ? 0xffff0000u : 0u);
        }
        u32x4 d0, d1, f0, f1;
#pragma unroll
        for (int r = 0; r < 4; ++r) {
            d0[r] = pk[r] & mk[r]; d1[r] = pk[4 + r] & mk[4 + r];
            f0[r] = pk[r] ^ d0[r]; f1[r] = pk[4 + r] ^ d1[r];
        }
        pv(d0, d1, 8);
        pv(f0, f1, 0);
    }
#pragma unroll
    for (int j = 1; j < 8; ++j) {
        const f32x16 s = MFMA32(kf[j], qf, negm);
        u32x4 p0, p1;
#pragma unroll
        for (int r = 0; r < 4; ++r) {
            p0[r] = cvt_pk_bf16(exp2_clamp(s[2 * r]), exp2_clamp(s[2 * r + 1]));
            p1[r] = cvt_pk_bf16(exp2_clamp(s[8 + 2 * r]), exp2_clamp(s[8 + 2 * r + 1]));
        }
        pv(p0, p1, j);
    }
    const float rl = __builtin_amdgcn_rcpf(o[8]);
    u32x4 pk = {cvt_pk_bf16(o[0] * rl, o[1] * rl), cvt_pk_bf16(o[2] * rl, o[3] * rl), cvt_pk_bf16(o[4] * rl, o[5] * rl), cvt_pk_bf16(o[6] * rl, o[7] * rl)};
    TS(4);
    *(u32x4*)(ws + WS_A2 + ((size_t)(qt * KSTEPS2 + head) * 64 + lane) * 16) = pk;
    TSOUT(1, 5);
}

constexpr int G2_WAVE_LDS = 16384;
constexpr int G2_LDS_BYTES = 8 * G2_WAVE_LDS;

DI void gload16(bf16x8& dst, const void* sbase, unsigned voff) {
    asm volatile("global_load_dwordx4 %0, %1, %2" : "=v"(dst) : "v"(voff), "s"(sbase) : "memory");
}
template <int N> DI void wait4(bf16x8& a, bf16x8& b, bf16x8& c, bf16x8& d) {
    asm volatile("s_waitcnt vmcnt(%4)" : "+v"(a), "+v"(b), "+v"(c), "+v"(d) : "n"(N) : "memory");
}

__global__ __launch_bounds__(512, 2) void k_gemm2(const __hip_bfloat16* __restrict__ x_, const __hip_bfloat16* __restrict__ wo_,
        const __hip_bfloat16* __restrict__ wd_, char* __restrict__ ws, __hip_bfloat16* __restrict__ out_) {
    const int b = blockIdx.x, xcd = b & 7, idx = b >> 3;
    TSDECL TS(0);
    const int tm = xcd * 4 + (idx >> 3), tn = idx & 7;
    const int tid = threadIdx.x, lane = tid & 63;
    const int wave = __builtin_amdgcn_readfirstlane(tid >> 6);
    const char* wd = (const char*)wd_ + (size_t)wave * 224 * 1024 + tn * 128;
    const char* wo = (const char*)wo_ + (size_t)wave * 64 * 1024 + tn * 128;
    const unsigned lbase = wave * G2_WAVE_LDS;
    const unsigned vsrc = (unsigned)((lane >> 3) * 1024 + (((lane & 7) ^ (((lane >> 4) & 1) << 2)) * 16));
    const int gq = lane >> 4, nb = gq & 1, hh = gq >> 1, r = (lane & 15) >> 2, p = lane & 3;
    const unsigned rd0 = lbase + (unsigned)(r * 128 + (((r >> 1) & 1) << 6) + nb * 32 + p * 8 + hh * 512);
    const unsigned rd1 = rd0 ^ 64;
    const char* a2h = ws + WS_A2 + (size_t)(tm * 2 * KSTEPS2 + 32 + 14 * wave) * 1024;
    const char* a2o = ws + WS_A2 + (size_t)(tm * 2 * KSTEPS2 + 4 * wave) * 1024;
    const unsigned va = lane * 16, vb = lane * 16 + KSTEPS2 * 1024;
    f32x16 acc[2][2] = {};
    bf16x8 ta[9][4];
    auto issueW = [&](int P) {
        const char* src = P < 7 ? wd + P * 32768 : wo + (P - 7) * 32768;
#pragma unroll
        for (int i = 0; i < 4; ++i) glds16(src + i * 8192, vsrc, lbase + (P & 3) * 4096 + i * 1024);
    };
    auto issueA = [&](int P) {
        const char* src = P < 7 ? a2h + P * 2048 : a2o + (P - 7) * 2048;
        gload16(ta[P][0], src, va); gload16(ta[P][1], src, vb); gload16(ta[P][2], src + 1024, va); gload16(ta[P][3], src + 1024, vb);
    };
    auto mma = [&](int P) {
        const unsigned so = (P & 3) * 4096;
        const unsigned e1 = P < 7 ? 1024 : 2048, t2 = P < 7 ? 2048 : 1024;
#pragma unroll
        for (int e = 0; e < 2; ++e) {
            const bf16x8 w0 = cat8(lds_tr(rd0 + so + e * e1), lds_tr(rd0 + so + e * e1 + t2));
            const bf16x8 w1 = cat8(lds_tr(rd1 + so + e * e1), lds_tr(rd1 + so + e * e1 + t2));
            acc[0][0] = MFMA32(w0, ta[P][2 * e], acc[0][0]); acc[0][1] = MFMA32(w0, ta[P][2 * e + 1], acc[0][1]);
            acc[1][0] = MFMA32(w1, ta[P][2 * e], acc[1][0]); acc[1][1] = MFMA32(w1, ta[P][2 * e + 1], acc[1][1]);
        }
    };
    issueW(0); issueA(0); issueW(1); issueA(1); issueW(2); issueA(2); issueW(3); issueA(3); issueA(4); issueA(5);
    TS(1);
#define G2_STEP(P, N) do { wait4<N>(ta[P][0], ta[P][1], ta[P][2], ta[P][3]); mma(P); SBAR(); asm volatile("s_waitcnt lgkmcnt(0)" ::: "memory"); \
        if (P + 4 < 9) issueW(P + 4); if (P + 6 < 9) issueA(P + 6); } while (0)
    G2_STEP(0, 32);
    TS(2);
    G2_STEP(1, 32);
    G2_STEP(2, 32);
    G2_STEP(3, 32);
    G2_STEP(4, 24);
    G2_STEP(5, 20);
    G2_STEP(6, 12);
    G2_STEP(7, 4);
    G2_STEP(8, 0);
    TS(3);
    __syncthreads();
    TS(4);
    {
        const int t = lane & 31, h2 = lane >> 5;
#pragma unroll
        for (int ct = 0; ct < 2; ++ct)
#pragma unroll
            for (int tt = 0; tt < 2; ++tt)
#pragma unroll
                for (int g4 = 0; g4 < 4; ++g4) {
                    const int tok = tt * 32 + t, chunk = ct * 8 + g4 * 2 + h2;
                    f32x4 v = {acc[ct][tt][4 * g4], acc[ct][tt][4 * g4 + 1], acc[ct][tt][4 * g4 + 2], acc[ct][tt][4 * g4 + 3]};
                    *(LAS f32x4*)(uintptr_t)(wave * 16384 + tok * 256 + ((chunk ^ (tok & 15)) * 16)) = v;
                }
    }
    TS(5);
    __syncthreads();
    TS(6);
    {
        const int tok = tid >> 3, c0 = (tid & 7) * 2;
        const size_t off = ((size_t)(tm * 64 + tok) * DM + tn * 64 + (tid & 7) * 8) * 2;
        const u32x4 xv = *(const u32x4*)((const char*)x_ + off);
        f32x4 s0 = {0.f, 0.f, 0.f, 0.f}, s1 = s0;
#pragma unroll
        for (int w = 0; w < 8; ++w) {
            s0 += *(const LAS f32x4*)(uintptr_t)(w * 16384 + tok * 256 + ((c0 ^ (tok & 15)) * 16));
            s1 += *(const LAS f32x4*)(uintptr_t)(w * 16384 + tok * 256 + (((c0 + 1) ^ (tok & 15)) * 16));
        }
        float xr[8];
#pragma unroll
        for (int i = 0; i < 4; ++i) { xr[2 * i] = __uint_as_float(xv[i] << 16); xr[2 * i + 1] = __uint_as_float(xv[i] & 0xffff0000u); }
        u32x4 o = {cvt_pk_bf16(s0[0] + xr[0], s0[1] + xr[1]), cvt_pk_bf16(s0[2] + xr[2], s0[3] + xr[3]),
                   cvt_pk_bf16(s1[0] + xr[4], s1[1] + xr[5]), cvt_pk_bf16(s1[2] + xr[6], s1[3] + xr[7])};
        *(u32x4*)((char*)out_ + off) = o;
    }
    TSOUT(2, 7);
}

extern "C" void kernel_launch(void* const* d_in, const int* in_sizes, int n_in, void* d_out, int out_size, void* d_ws, size_t ws_size,
                              hipStream_t stream) {
    const __hip_bfloat16* x = (const __hip_bfloat16*)d_in[0];
    const __hip_bfloat16* wqkv = (const __hip_bfloat16*)d_in[1];
    const __hip_bfloat16* wo = (const __hip_bfloat16*)d_in[2];
    const __hip_bfloat16* wg = (const __hip_bfloat16*)d_in[4];
    const __hip_bfloat16* wu = (const __hip_bfloat16*)d_in[5];
    const __hip_bfloat16* wd = (const __hip_bfloat16*)d_in[6];
    (void)hipFuncSetAttribute((const void*)k_gemm1, hipFuncAttributeMaxDynamicSharedMemorySize, G1_LDS_BYTES);
    (void)hipFuncSetAttribute((const void*)k_gemm2, hipFuncAttributeMaxDynamicSharedMemorySize, G2_LDS_BYTES);
    k_gemm1<<<256, 512, G1_LDS_BYTES, stream>>>(x, wqkv, wg, wu, (char*)d_ws);
    k_attn<<<256, 512, AT_LDS_BYTES, stream>>>((char*)d_ws);
    k_gemm2<<<256, 512, G2_LDS_BYTES, stream>>>(x, wo, wd, (char*)d_ws, (__hip_bfloat16*)d_out);
}
```

```cpp
#include <hip/hip_runtime.h>
#include <hip/hip_bf16.h>
#include <stdint.h>

typedef short bf16x8 __attribute__((ext_vector_type(8)));
typedef short s16x4 __attribute__((ext_vector_type(4)));
typedef float f32x2 __attribute__((ext_vector_type(2)));
typedef float f32x4 __attribute__((ext_vector_type(4)));
typedef float f32x16 __attribute__((ext_vector_type(16)));
typedef unsigned u32x4 __attribute__((ext_vector_type(4)));
typedef unsigned u32x2 __attribute__((ext_vector_type(2)));
typedef __bf16 bf16x2_t __attribute__((ext_vector_type(2)));
typedef unsigned long long u64;
#define LAS __attribute__((address_space(3)))
#define DI __device__ __forceinline__

constexpr int SEQ = 2048, DM = 512;
constexpr int KSTEPS2 = 144;
constexpr unsigned WS_EPOCH = 0;
constexpr unsigned WS_F1 = 4096;
constexpr unsigned WS_F2 = WS_F1 + 16384;
constexpr unsigned WS_F3 = WS_F2 + 16384;
constexpr unsigned WS_FO = WS_F3 + 16384;
constexpr unsigned WS_QKV = 131072;
constexpr unsigned WS_A2  = WS_QKV + 6291456;
constexpr unsigned WS_DUMMY = WS_A2 + 9437184;
constexpr unsigned SPIN_CAP = 1u << 14;

DI unsigned cvt_pk_bf16(float lo, float hi) {
    f32x2 v = {lo, hi};
    bf16x2_t r = __builtin_convertvector(v, bf16x2_t);
    return __builtin_bit_cast(unsigned, r);
}
DI float dot2_bf16(unsigned a, unsigned b, float c) {
    return __builtin_amdgcn_fdot2_f32_bf16(__builtin_bit_cast(bf16x2_t, a), __builtin_bit_cast(bf16x2_t, b), c, false);
}
DI void glds16(const void* sbase, unsigned voff, unsigned ldsaddr) {
    asm volatile("s_mov_b32 m0, %2\n\ts_nop 0\n\tglobal_load_lds_dwordx4 %0, %1"
                 :: "v"(voff), "s"(sbase), "s"(ldsaddr) : "memory");
}
template <int IMM> DI void glds16i(const void* sbase, unsigned voff, unsigned ldsbase) {
    asm volatile("" : "+s"(ldsbase));
    glds16(sbase, voff, ldsbase + IMM);
}
DI const char* spin(const char* p) { asm volatile("" : "+s"(p)); return p; }
template <int OFF, class T> DI void gload16(T& dst, const void* sbase, unsigned voff) {
    asm volatile("global_load_dwordx4 %0, %1, %2 offset:%3" : "=v"(dst) : "v"(voff), "s"(sbase), "n"(OFF) : "memory");
}
template <int OFF, class T> DI void gload16_dev(T& dst, const void* sbase, unsigned voff) {
    asm volatile("global_load_dwordx4 %0, %1, %2 offset:%3 sc1" : "=v"(dst) : "v"(voff), "s"(sbase), "n"(OFF) : "memory");
}
DI void gload8_dev(u32x2& dst, const void* sbase, unsigned voff) {
    asm volatile("global_load_dwordx2 %0, %1, %2 sc1" : "=v"(dst) : "v"(voff), "s"(sbase) : "memory");
}
DI void gstore16_dev(const void* sbase, unsigned voff, u32x4 v) {
    asm volatile("global_store_dwordx4 %0, %1, %2 sc1\n\ts_nop 1" :: "v"(voff), "v"(v), "s"(sbase) : "memory");
}
DI void gstore8_dev(const void* sbase, unsigned voff, u32x2 v) {
    asm volatile("global_store_dwordx2 %0, %1, %2 sc1" :: "v"(voff), "v"(v), "s"(sbase) : "memory");
}
template <int N, class T> DI void wait4(T& a, T& b, T& c, T& d) {
    asm volatile("s_waitcnt vmcnt(%4)" : "+v"(a), "+v"(b), "+v"(c), "+v"(d) : "n"(N) : "memory");
}
template <int N, class T> DI void wait1(T& a) { asm volatile("s_waitcnt vmcnt(%1)" : "+v"(a) : "n"(N) : "memory"); }
#define WAITV(N) asm volatile("s_waitcnt vmcnt(" #N ")" ::: "memory")
DI bf16x8 lds_b128(unsigned addr) { return *(const LAS bf16x8*)(uintptr_t)addr; }
DI void lds_st128(unsigned addr, u32x4 v) { *(LAS u32x4*)(uintptr_t)addr = v; }
DI s16x4 lds_tr(unsigned addr) { return __builtin_amdgcn_ds_read_tr16_b64_v4i16((LAS s16x4*)(uintptr_t)addr); }
DI bf16x8 cat8(s16x4 a, s16x4 b) { return __builtin_shufflevector(a, b, 0, 1, 2, 3, 4, 5, 6, 7); }
template <int N> DI void waitv_bar() { asm volatile("s_waitcnt vmcnt(%0) lgkmcnt(0)\n\ts_barrier" :: "n"(N) : "memory"); }
#define LGKM_BAR() asm volatile("s_waitcnt lgkmcnt(0)\n\ts_barrier" ::: "memory")
#define MFMA16(a, b, c) __builtin_amdgcn_mfma_f32_16x16x32_bf16(a, b, c, 0, 0, 0)
#define MFMA32(a, b, c) __builtin_amdgcn_mfma_f32_32x32x16_bf16(a, b, c, 0, 0, 0)
#define SBAR() __builtin_amdgcn_sched_barrier(0)
#define PIN(x) asm volatile("" : "+v"(x))

#define TS(i)
#define TSDECL
#define TSOUT(kid,n)

constexpr int LX = 0, XSLOT = 16384, LW = 65536, WSL = 8192;
constexpr int AT_LDS_V = 0, AT_LDS_K = 65536;
constexpr int G2_S0 = 98304, G2_S1 = 131072, G2_S2 = 32768;
constexpr int LDS_BYTES = 163840;

constexpr float QSCALE = 0.25f * 1.4426950408889634f;
constexpr float GC1 = -2.f * 0.7978845608028654f * 1.4426950408889634f, GC2 = GC1 * 0.044715f;

struct G1 {
    const char* sb[5];
    unsigned v0, vM, vF;
    unsigned stepM;
    unsigned wave1k, wave4k;
    const char* wd;
    unsigned vsrc2;
    unsigned xa[2], wa[2], xw;
    u32x4 xr[2][8];
    float rstd[2], nmr[2];
    f32x4 acc[5][4];
    float t0, t1, p0, p1;
    unsigned hp[4];
    const char* hdst;
    const char* ws; unsigned myflag, fq_off; u32x2 tagv, pq; int lane;
};
struct Frag { bf16x8 x[4]; s16x4 w[3][2]; };

template <int S> DI void g1_issue(G1& c) {
    constexpr unsigned dst = LW + (S % 12) * WSL;
    if constexpr (S < 24) {
        constexpr int u = S % 3;
        glds16i<dst>(c.sb[u], u == 0 ? c.v0 : c.vM, c.wave1k);
        if constexpr (u == 0) { c.v0 += 64 * 3072u; PIN(c.v0); }
        if constexpr (u == 2) { c.vM += c.stepM; PIN(c.vM); }
    } else {
        constexpr int n = (S - 24) >> 1, u = (S - 24) & 1;
        glds16i<dst>(c.sb[3 + u], c.vF, c.wave1k);
        if constexpr (u == 1) { c.vF += (n & 1) ? (unsigned)(-3 * 64 * 3584) : 64 * 3584u; PIN(c.vF); }
    }
}
template <int S, int e, int T> DI s16x4 g1_wrd(const G1& c) {
    constexpr unsigned off = (S % 12) * WSL + e * 4096 + T * 512;
    return lds_tr(c.wa[off >> 16] + (off & 0xffff));
}
template <int I, int e, int R> DI void p1_rd(Frag& f, const G1& c) {
    if constexpr (R < 2) f.w[0][R] = g1_wrd<3 * I, e, R>(c);
    else if constexpr (R < 6) f.x[R - 2] = lds_b128(c.xa[e] + LX + (I & 3) * XSLOT + (R - 2) * 2048);
    else f.w[(R - 4) >> 1][R & 1] = g1_wrd<3 * I + ((R - 4) >> 1), e, (R & 1)>(c);
}
template <int N, int R> DI void p2_rd(Frag& f, const G1& c) {
    constexpr int n = N >> 1, e = N & 1, slice = 6 - 2 * (n >> 1) + (n & 1);
    if constexpr (R < 2) f.w[0][R] = g1_wrd<24 + 2 * n, e, R>(c);
    else if constexpr (R < 6) f.x[R - 2] = lds_b128(c.xa[e] + LX + (slice & 3) * XSLOT + (R - 2) * 2048);
    else f.w[1][R & 1] = g1_wrd<25 + 2 * n, e, (R & 1)>(c);
}
template <int J0, int M> DI void g1_mm(G1& c, const Frag& f) {
    constexpr int j = M >> 2, i = M & 3;
    c.acc[J0 + j][i] = MFMA16(cat8(f.w[j][0], f.w[j][1]), f.x[i], c.acc[J0 + j][i]);
    PIN(c.acc[J0 + j][i]);
}
template <int I, int row> DI void g1_xwrite(G1& c) { lds_st128((row == 0 ? c.xw : (c.xw ^ 64) + 1024) + LX + (I & 3) * XSLOT, c.xr[row][I]); }
template <int I, int OP> DI void g1_normop(G1& c) {
    constexpr int u = OP / 5, o = OP % 5, row = u >> 2, r = u & 3;
    if constexpr (o == 0) { c.t0 = __uint_as_float(c.xr[row][I][r] << 16); PIN(c.t0); }
    if constexpr (o == 1) { c.t1 = __uint_as_float(c.xr[row][I][r] & 0xffff0000u); PIN(c.t1); }
    if constexpr (o == 2) { c.t0 = __builtin_fmaf(c.t0, c.rstd[row], c.nmr[row]); asm volatile("" : "+v"(c.t0), "+v"(c.t1)); }
    if constexpr (o == 3) { c.t1 = __builtin_fmaf(c.t1, c.rstd[row], c.nmr[row]); PIN(c.t1); }
    if constexpr (o == 4) {
        unsigned w = cvt_pk_bf16(c.t0, c.t1); PIN(w);
        c.xr[row][I][r] = w;
        if constexpr (r == 3) g1_xwrite<I, row>(c);
    }
}
template <int JG, int OP> DI void g1_geluop(G1& c, const char* dst) {
    constexpr int pr = OP / 18, o = OP % 18, i = pr >> 1, r = (pr & 1) * 2;
    const float g0 = c.acc[JG][i][r], g1 = c.acc[JG][i][r + 1];
#define P2 asm volatile("" : "+v"(c.t0), "+v"(c.t1))
    if constexpr (o == 0) { c.t0 = g0 * g0; PIN(c.t0); }
    if constexpr (o == 1) { c.t1 = g1 * g1; P2; }
    if constexpr (o == 2) { c.t0 = __builtin_fmaf(c.t0, GC2, GC1); P2; }
    if constexpr (o == 3) { c.t1 = __builtin_fmaf(c.t1, GC2, GC1); P2; }
    if constexpr (o == 4) { c.t0 = c.t0 * g0; P2; }
    if constexpr (o == 5) { c.t1 = c.t1 * g1; P2; }
    if constexpr (o == 6) { c.t0 = __builtin_amdgcn_exp2f(c.t0); P2; }
    if constexpr (o == 7) { c.p0 = g0 * c.acc[JG + 1][i][r]; PIN(c.p0); }
    if constexpr (o == 8) { c.t1 = __builtin_amdgcn_exp2f(c.t1); P2; }
    if constexpr (o == 9) { c.p1 = g1 * c.acc[JG + 1][i][r + 1]; asm volatile("" : "+v"(c.p0), "+v"(c.p1)); }
    if constexpr (o == 10) { c.t0 = 1.f + c.t0; P2; }
    if constexpr (o == 11) { c.t1 = 1.f + c.t1; P2; }
    if constexpr (o == 12) { c.t0 = __builtin_amdgcn_rcpf(c.t0); P2; }
    if constexpr (o == 13) { c.t1 = __builtin_amdgcn_rcpf(c.t1); P2; }
    if constexpr (o == 14) { c.p0 = c.p0 * c.t0; asm volatile("" : "+v"(c.p0), "+v"(c.p1)); }
    if constexpr (o == 15) { c.p1 = c.p1 * c.t1; PIN(c.p1); }
    if constexpr (o == 16) { c.hp[pr & 3] = cvt_pk_bf16(c.p0, c.p1); PIN(c.hp[pr & 3]); }
    if constexpr (o == 17 && (pr & 3) == 3) {
        const auto s0 = __builtin_amdgcn_permlane16_swap(c.hp[0], c.hp[2], false, false);
        const auto s1 = __builtin_amdgcn_permlane16_swap(c.hp[1], c.hp[3], false, false);
        gstore16_dev(dst, (unsigned)(c.lane * 16 + (pr >> 2) * (KSTEPS2 * 1024)), u32x4{s0[0], s1[0], s0[1], s1[1]});
    }
#undef P2
}
template <int I, int G> DI void p1_gap(G1& c, Frag& f0, Frag& f1) {
    constexpr int h = G / 12, k = G % 12;
    Frag& fm = h == 0 ? f1 : f0;
    Frag& fr = h == 0 ? f0 : f1;
    if constexpr (G % 6 == 0) __builtin_amdgcn_s_setprio(3 - G / 6);
    if constexpr (!(I == 0 && h == 0)) g1_mm<0, k>(c, fm);
    SBAR();
    if constexpr (k < 10) p1_rd<I, h, k>(fr, c);
    if constexpr (I + 1 < 8 && G < 20) { g1_normop<I + 1, 2 * G>(c); g1_normop<I + 1, 2 * G + 1>(c); }
    if constexpr ((G % 8) == 7) g1_issue<3 * I + 9 + G / 8>(c);
    SBAR();
}
template <int I> DI void p1_interval(G1& c, Frag& f0, Frag& f1) {
#define CH(C) p1_gap<I, C>(c, f0, f1)
#define CH6(B) CH(B); CH(B + 1); CH(B + 2); CH(B + 3); CH(B + 4); CH(B + 5);
    CH6(0) CH6(6) CH6(12) CH6(18)
#undef CH
#undef CH6
    waitv_bar<(I < 7 ? 6 : 5)>();
}
template <int J, int G> DI void p2_gap(G1& c, Frag& f0, Frag& f1) {
    constexpr int qd = G / 8, k = G % 8, N = 4 * J + qd;
    Frag& fm = (qd & 1) == 0 ? f1 : f0;
    Frag& fr = (qd & 1) == 0 ? f0 : f1;
    if constexpr (k == 0) __builtin_amdgcn_s_setprio(3 - qd);
    if constexpr (N > 0) g1_mm<3, k>(c, fm);
    SBAR();
    if constexpr (N > 0) p2_rd<N, k>(fr, c);
    constexpr int gg = 32 * J + G - 8;
    if constexpr (gg >= 0 && gg < 72) { g1_geluop<1, 2 * gg>(c, c.hdst); g1_geluop<1, 2 * gg + 1>(c, c.hdst); }
    if constexpr (J == 1 && (G % 8) == 3) g1_issue<36 + G / 8>(c);
    if constexpr ((J == 1 || J == 2) && (G % 8) == 6) g1_xwrite<(J == 1 ? 2 : 0) + (G >> 4), (G >> 3) & 1>(c);
    if constexpr (J == 2 && G == 0) gstore8_dev(c.ws, WS_F1 + c.myflag, c.tagv);
    if constexpr (J == 2 && G >= 20 && (G % 3) == 2) glds16i<G2_S0 + ((G - 20) / 3) * 1024>(spin(c.wd) + ((G - 20) / 3) * 8192, c.vsrc2, c.wave4k);
    if constexpr (J == 3 && G == 8) gload8_dev(c.pq, c.ws, c.fq_off);
    if constexpr (J == 3 && G >= 9 && G < 31 && (G % 3) == 0) {
        constexpr int n = (G - 9) / 3;
        glds16i<(n < 4 ? G2_S1 : G2_S2) + (n & 3) * 1024>(spin(c.wd) + (n < 4 ? 131072 : 262144) + (n & 3) * 8192, c.vsrc2, c.wave4k);
    }
    SBAR();
}
template <int J, int G0> DI void p2_interval(G1& c, Frag& f0, Frag& f1) {
#define CH(C) p2_gap<J, C>(c, f0, f1)
#define CH8(B) CH(B); CH(B + 1); CH(B + 2); CH(B + 3); CH(B + 4); CH(B + 5); CH(B + 6); CH(B + 7);
    if constexpr (G0 == 0) { CH8(0) }
    CH8(8) CH8(16) CH8(24)
#undef CH
#undef CH8
}
DI float dpp_sum8(float v) {
    v += __builtin_bit_cast(float, __builtin_amdgcn_update_dpp(0, __builtin_bit_cast(int, v), 0xB1, 0xf, 0xf, true));
    v += __builtin_bit_cast(float, __builtin_amdgcn_update_dpp(0, __builtin_bit_cast(int, v), 0x4E, 0xf, 0xf, true));
    v += __builtin_bit_cast(float, __builtin_amdgcn_update_dpp(0, __builtin_bit_cast(int, v), 0x141, 0xf, 0xf, true));
    return v;
}
DI float exp2_clamp(float v) { return __builtin_amdgcn_fmed3f(__builtin_amdgcn_exp2f(v), 0.f, 1.f); }
DI float max3(float a, float b, float c) { return __builtin_fmaxf(__builtin_fmaxf(a, b), c); }
DI bool vis(int R) {
    const int bb = (R & 3) + 8 * (R >> 2);
    const u64 lo = 0xffffffffull & (0xffffffffull << bb), hi = 0xffffffffull & (0xffffffffull << (bb + 4));
    return __builtin_amdgcn_inverse_ballot_w64(lo | (hi << 32));
}
DI void poll_flags(const char* ws, unsigned foff, u32x2 v, u32x2 tag) {
#pragma clang loop unroll(disable)
    for (unsigned spins = 0; spins < SPIN_CAP; ++spins) {
        if (__all(v[0] == tag[0] && v[1] == tag[1])) break;
        __builtin_amdgcn_s_sleep(2);
        gload8_dev(v, ws, foff); wait1<0>(v);
    }
}

__global__ __launch_bounds__(512, 2) void k_fused(const __hip_bfloat16* __restrict__ x_, const __hip_bfloat16* __restrict__ wqkv_,
        const __hip_bfloat16* __restrict__ wo_, const __hip_bfloat16* __restrict__ wg_, const __hip_bfloat16* __restrict__ wu_,
        const __hip_bfloat16* __restrict__ wd_, char* __restrict__ ws, __hip_bfloat16* __restrict__ out_) {
    const int b = blockIdx.x, xcd = b & 7, idx = b >> 3;
    TSDECL TS(0);
    const int tid = threadIdx.x, lane = tid & 63;
    const int wave = __builtin_amdgcn_readfirstlane(tid >> 6);
    const int tm2 = xcd * 4 + (idx >> 3), tn2 = idx & 7;
    const int head = xcd * 4 + (idx & 3), tg = idx >> 2, qt = 8 * tg + wave;
    const int tm = (xcd >> 2) * 8 + (idx >> 2), cq = xcd & 3, cp = idx & 3, tn = cq * 4 + cp;
    const int wr = wave >> 2, wc = wave & 3;
    const bool typeA = cp < 3;
    const int qu0 = 6 * cq + cp, f1 = 7 * cq + 2 * cp, f2 = f1 + (typeA ? 1 : 0);
    const int tok0 = tm * 128 + 64 * wr;
    G1 c; c.wave1k = wave * 1024; c.wave4k = wave * 4096; c.ws = ws; c.lane = lane;
    u32x2 ep; gload8_dev(ep, ws, WS_EPOCH);
    {
        const char* xp = (const char*)x_ + ((size_t)tm * 128 + 16 * wave) * 1024;
        const unsigned v0 = (lane >> 3) * 1024 + (lane & 7) * 16, v1 = v0 + 8192;
        gload16<0>(c.xr[0][0], xp, v0);   gload16<0>(c.xr[1][0], xp, v1);   gload16<128>(c.xr[0][1], xp, v0); gload16<128>(c.xr[1][1], xp, v1);
        gload16<256>(c.xr[0][2], xp, v0); gload16<256>(c.xr[1][2], xp, v1); gload16<384>(c.xr[0][3], xp, v0); gload16<384>(c.xr[1][3], xp, v1);
        gload16<512>(c.xr[0][4], xp, v0); gload16<512>(c.xr[1][4], xp, v1); gload16<640>(c.xr[0][5], xp, v0); gload16<640>(c.xr[1][5], xp, v1);
        gload16<768>(c.xr[0][6], xp, v0); gload16<768>(c.xr[1][6], xp, v1); gload16<896>(c.xr[0][7], xp, v0); gload16<896>(c.xr[1][7], xp, v1);
    }
    {
        const char* wqkv = (const char*)wqkv_; const char* wg = (const char*)wg_; const char* wu = (const char*)wu_;
        const unsigned strM = typeA ? 3584u : 3072u;
        const int r = lane >> 3, ch = lane & 7;
        const int g = ((r >> 1) & 1) | ((wave & 1) << 1);
        const unsigned colw = (unsigned)((ch ^ (g << 1)) * 16);
        c.v0 = (8 * wave + r) * 3072u + colw; c.vM = (8 * wave + r) * strM + colw; c.vF = (384 + 8 * wave + r) * 3584u + colw;
        c.stepM = 64 * strM;
        c.sb[0] = wqkv + qu0 * 128;
        c.sb[1] = typeA ? wg + f1 * 128 : c.sb[0] + 128;
        c.sb[2] = typeA ? wu + f1 * 128 : c.sb[0] + 256;
        c.sb[3] = wg + f2 * 128;
        c.sb[4] = wu + f2 * 128;
        c.wd = (const char*)wd_ + (size_t)((wave >> 1) * 448 + (wave & 1) * 32) * 1024 + tn2 * 128;
        c.vsrc2 = (unsigned)((lane >> 3) * 1024 + (((lane & 7) ^ (((lane >> 4) & 1) << 2)) * 16));
        c.hdst = typeA ? ws + WS_A2 + (size_t)((tok0 >> 5) * KSTEPS2 + 32 + 4 * f1 + wc) * 1024 : ws + WS_DUMMY + ((tm * 4 + cq) * 8 + wave) * 1024;
        c.myflag = (unsigned)(((tm * 16 + tn) * 8 + wave) * 8);
    }
    {
        const int t = lane & 15, q = lane >> 4;
        c.xa[0] = (unsigned)((64 * wr + t) * 128 + ((q ^ (t >> 1)) * 16));
        c.xa[1] = c.xa[0] ^ 64;
        const int r = (lane & 15) >> 2, p = lane & 3;
        const int g = ((r >> 1) & 1) | ((q & 1) << 1);
        c.wa[0] = (unsigned)(LW + (8 * q + r) * 128 + (p >> 1) * 16 + (p & 1) * 8 + ((wc ^ g) << 5));
        c.wa[1] = c.wa[0] + 65536; PIN(c.wa[0]); PIN(c.wa[1]);
        const int row = 16 * wave + (lane >> 3);
        c.xw = (unsigned)(row * 128 + (((lane & 7) ^ ((row >> 1) & 7)) * 16));
        const int sel = lane < 5 ? lane : 0;
        const int which = sel == 0 ? 0 : 2 - (sel & 1);
        const int T = (sel >= 3 && tg > 0) ? qt - 8 : qt;
        const int u = which * 8 + (head >> 2);
        const int pq = (u * 43) >> 8, pr = u - 6 * pq;
        c.fq_off = WS_F1 + (((T >> 2) * 16 + 4 * pq + (pr < 3 ? pr : 3)) * 8 + ((T >> 1) & 1) * 4 + (head & 3)) * 8;
    }
    TS(1);
    {
        const unsigned ones = 0x3f803f80u;
        float s1[2][2] = {}, s2[2][2] = {};
        auto stat = [&](int s) {
#pragma unroll
            for (int h = 0; h < 2; ++h)
#pragma unroll
                for (int r = 0; r < 4; ++r) {
                    s1[h][r & 1] = dot2_bf16(c.xr[h][s][r], ones, s1[h][r & 1]);
                    s2[h][r & 1] = dot2_bf16(c.xr[h][s][r], c.xr[h][s][r], s2[h][r & 1]);
                }
            asm volatile("" : "+v"(s1[0][0]), "+v"(s1[0][1]), "+v"(s1[1][0]), "+v"(s1[1][1]), "+v"(s2[0][0]), "+v"(s2[0][1]), "+v"(s2[1][0]), "+v"(s2[1][1]));
        };
        wait4<12>(c.xr[0][0], c.xr[1][0], c.xr[0][1], c.xr[1][1]); PIN(ep); stat(0); stat(1);
        wait4<8>(c.xr[0][2], c.xr[1][2], c.xr[0][3], c.xr[1][3]);  stat(2); stat(3);
        g1_issue<0>(c); g1_issue<1>(c); g1_issue<2>(c);
        wait4<7>(c.xr[0][4], c.xr[1][4], c.xr[0][5], c.xr[1][5]);  stat(4); stat(5);
        wait4<3>(c.xr[0][6], c.xr[1][6], c.xr[0][7], c.xr[1][7]);  stat(6); stat(7);
        g1_issue<3>(c); g1_issue<4>(c); g1_issue<5>(c);
#pragma unroll
        for (int h = 0; h < 2; ++h) {
            const float t1 = dpp_sum8(s1[h][0] + s1[h][1]), t2 = dpp_sum8(s2[h][0] + s2[h][1]);
            const float mean = t1 * (1.f / 512.f);
            const float var = fmaxf(t2 * (1.f / 512.f) - mean * mean, 0.f) + 1e-5f;
            c.rstd[h] = __builtin_amdgcn_rsqf(var); c.nmr[h] = -mean * c.rstd[h];
        }
    }
    {
        u64 z = ((u64)(unsigned)__builtin_amdgcn_readfirstlane(ep[1]) << 32 | (unsigned)__builtin_amdgcn_readfirstlane(ep[0])) + 0x9E3779B97F4A7C15ull;
        z = (z ^ (z >> 30)) * 0xBF58476D1CE4E5B9ull; z = (z ^ (z >> 27)) * 0x94D049BB133111EBull; z ^= z >> 31;
        c.tagv = u32x2{(unsigned)z, (unsigned)(z >> 32)};
    }
    TS(2);
#define NO(O) g1_normop<0, O>(c)
#define NO10(B) NO(B); NO(B + 1); NO(B + 2); NO(B + 3); NO(B + 4); NO(B + 5); NO(B + 6); NO(B + 7); NO(B + 8); NO(B + 9);
    NO10(0) NO10(10) NO10(20) NO10(30)
#undef NO
#undef NO10
    g1_issue<6>(c); g1_issue<7>(c); g1_issue<8>(c);
#pragma unroll
    for (int j = 0; j < 5; ++j)
#pragma unroll
        for (int i = 0; i < 4; ++i) c.acc[j][i] = f32x4{0.f, 0.f, 0.f, 0.f};
    Frag f0, f1_;
    waitv_bar<6>();
    TS(3);
    p1_interval<0>(c, f0, f1_); p1_interval<1>(c, f0, f1_); p1_interval<2>(c, f0, f1_); p1_interval<3>(c, f0, f1_);
    TS(4);
    p1_interval<4>(c, f0, f1_); p1_interval<5>(c, f0, f1_); p1_interval<6>(c, f0, f1_); p1_interval<7>(c, f0, f1_);
    TS(5);
    {
        __builtin_amdgcn_s_setprio(3);
#define LG(K) g1_mm<0, K>(c, f1_); SBAR(); if constexpr (K < 8) p2_rd<0, (K < 8 ? K : 0)>(f0, c); if constexpr (K >= 8 && K < 11) g1_issue<33 + (K >= 8 && K < 11 ? K - 8 : 0)>(c); SBAR();
        LG(0) LG(1) LG(2) LG(3) LG(4) LG(5) LG(6) LG(7) LG(8) LG(9) LG(10) LG(11)
#undef LG
        const int t = lane & 15, q = lane >> 4;
        const unsigned qoff = (unsigned)((tok0 + (q & 1) * 16 + t) * 32 + (q >> 1) * 16);
        auto store_qkv = [&](int j) {
            const int hcol = (qu0 + j) * 4 + wc;
            const float sc = hcol < 32 ? QSCALE : 1.f;
            const char* dst = ws + WS_QKV + (size_t)hcol * SEQ * 32;
#pragma unroll
            for (int i = 0; i < 4; i += 2) {
                const unsigned a0 = cvt_pk_bf16(c.acc[j][i][0] * sc, c.acc[j][i][1] * sc), a1 = cvt_pk_bf16(c.acc[j][i][2] * sc, c.acc[j][i][3] * sc);
                const unsigned b0 = cvt_pk_bf16(c.acc[j][i + 1][0] * sc, c.acc[j][i + 1][1] * sc), b1 = cvt_pk_bf16(c.acc[j][i + 1][2] * sc, c.acc[j][i + 1][3] * sc);
                const auto s0 = __builtin_amdgcn_permlane16_swap(a0, b0, false, false);
                const auto s1 = __builtin_amdgcn_permlane16_swap(a1, b1, false, false);
                gstore16_dev(dst, qoff + i * 512, u32x4{s0[0], s1[0], s0[1], s1[1]});
            }
        };
        store_qkv(0);
        if (!typeA) { store_qkv(1); store_qkv(2); }
        PIN(c.xa[0]); PIN(c.xa[1]); PIN(c.wa[0]); PIN(c.wa[1]); PIN(c.xw);
    }
    p2_interval<0, 8>(c, f0, f1_); waitv_bar<6>();
    p2_interval<1, 0>(c, f0, f1_); waitv_bar<5>();
    TS(6);
    p2_interval<2, 0>(c, f0, f1_); waitv_bar<6>();
    p2_interval<3, 0>(c, f0, f1_); LGKM_BAR();
    g1_mm<3, 0>(c, f1_); g1_mm<3, 1>(c, f1_); g1_mm<3, 2>(c, f1_); g1_mm<3, 3>(c, f1_); g1_mm<3, 4>(c, f1_); g1_mm<3, 5>(c, f1_); g1_mm<3, 6>(c, f1_); g1_mm<3, 7>(c, f1_);
    __builtin_amdgcn_s_setprio(0);
    TS(7);
    const u32x2 tagv = c.tagv;
    wait1<8>(c.pq);
    gstore8_dev(ws, WS_F2 + c.myflag, tagv);
    poll_flags(ws, c.fq_off, c.pq, tagv);
    TS(8);
    unsigned fe_off, fl_off, fo_off;
    {
        const int pb = ((tm2 >> 1) * 16 + (wave >> 1) * 4) * 8 + (tm2 & 1) * 4 + (wave & 1) * 2 + (lane & 1);
        const int cpe = (lane >> 1) < 3 ? (lane >> 1) : 0, cpl = (lane >> 1) & 3;
        fe_off = WS_F2 + (pb + cpe * 8) * 8;
        fl_off = WS_F3 + (pb + cpl * 8) * 8;
        if (b == 0 && lane >= 32) fl_off = WS_F1 + (wave * 32 + lane - 32) * 64;
        fo_off = WS_FO + ((2 * tm2 + ((lane >> 2) & 1)) * 32 + 4 * wave + (lane & 3)) * 8;
    }
    const char* a2h = ws + WS_A2 + (size_t)(tm2 * 2 * KSTEPS2 + 32 + 28 * (wave >> 1) + 2 * (wave & 1)) * 1024;
    const char* a2o = ws + WS_A2 + (size_t)(tm2 * 2 * KSTEPS2 + 4 * wave) * 1024;
    const unsigned va2 = lane * 16, vb2 = lane * 16 + KSTEPS2 * 1024;
    bf16x8 ta[9][4];
    auto unit_of = [](int P) { return P < 3 ? 2 * P : (P < 6 ? 2 * (P - 3) + 1 : 6); };
    auto issueA = [&](int P) {
        const char* src = P < 7 ? spin(a2h) + unit_of(P) * 4096 : spin(a2o) + (P - 7) * 2048;
        gload16_dev<0>(ta[P][0], src, va2); gload16_dev<0>(ta[P][1], src, vb2); gload16_dev<1024>(ta[P][2], src, va2); gload16_dev<1024>(ta[P][3], src, vb2);
    };
    {
        const char* Q = ws + WS_QKV + (size_t)head * SEQ * 32;
        const char* K = Q + (size_t)32 * SEQ * 32;
        const char* V = K + (size_t)32 * SEQ * 32;
        const int a = lane & 31, hh = lane >> 5;
        bf16x8 qf;
        {
            const bool lo_ok = tg > 0;
            const int kt0 = lo_ok ? qt - 8 : qt;
            const unsigned vk = a * 32 + hh * 16, vv = lane * 16;
            u32x4 k0, k1, v0, v1;
            gload16_dev<0>(k0, K + kt0 * 1024, vk); gload16_dev<0>(k1, K + qt * 1024, vk);
            gload16_dev<0>(v0, V + kt0 * 1024, vv); gload16_dev<0>(v1, V + qt * 1024, vv);
            gload16_dev<0>(qf, Q + qt * 1024, vk);
            {
                const char* dst = ws + WS_A2 + (size_t)((tok0 >> 5) * KSTEPS2 + 32 + 4 * f2 + wc) * 1024;
                unsigned hp[8];
#pragma unroll
                for (int i = 0; i < 4; ++i)
#pragma unroll
                    for (int r = 0; r < 4; r += 2) {
                        float h[2];
#pragma unroll
                        for (int z = 0; z < 2; ++z) {
                            const float ga = c.acc[3][i][r + z], ua = c.acc[4][i][r + z];
                            const float e = __builtin_amdgcn_exp2f(ga * __builtin_fmaf(ga * ga, GC2, GC1));
                            h[z] = ga * ua * __builtin_amdgcn_rcpf(1.f + e);
                        }
                        hp[2 * i + (r >> 1)] = cvt_pk_bf16(h[0], h[1]);
                    }
#pragma unroll
                for (int i = 0; i < 2; ++i) {
                    const auto s0 = __builtin_amdgcn_permlane16_swap(hp[4 * i], hp[4 * i + 2], false, false);
                    const auto s1 = __builtin_amdgcn_permlane16_swap(hp[4 * i + 1], hp[4 * i + 3], false, false);
                    gstore16_dev(dst, (unsigned)(lane * 16 + i * (KSTEPS2 * 1024)), u32x4{s0[0], s1[0], s0[1], s1[1]});
                }
            }
            TS(9);
            const unsigned one = (lane & 1) ? 0u : 0x3f80u;
            u32x4 c1 = {one, 0u, one, 0u}, c0 = c1;
            if (!lo_ok) c0 = u32x4{0u, 0u, 0u, 0u};
            const unsigned va = AT_LDS_V + wave * 2048 + (lane >> 1) * 64 + (lane & 1) * 16;
            lds_st128(va + 32, c0); lds_st128(va + 16384 + 32, c1);
            wait4<3>(k0, k1, v0, v1);
            if (!lo_ok) v0 = u32x4{0u, 0u, 0u, 0u};
            lds_st128(AT_LDS_K + wave * 1024 + lane * 16, k0); lds_st128(AT_LDS_K + 8192 + wave * 1024 + lane * 16, k1);
            lds_st128(va, v0); lds_st128(va + 16384, v1);
            wait1<2>(qf);
        }
        u32x2 fv; gload8_dev(fv, ws, fe_off);
        LGKM_BAR();
        TS(10);
        const unsigned kaddr = AT_LDS_K + wave * 1024 + lane * 16;
        bf16x8 kf[9];
#pragma unroll
        for (int j = 0; j < 9; ++j) kf[j] = lds_b128(kaddr + j * 1024);
        const f32x16 zero = {};
        f32x16 sm;
        {
            const f32x16 sf = MFMA32(kf[0], qf, zero);
            const f32x16 sd = MFMA32(kf[8], qf, zero);
#pragma unroll
            for (int r = 0; r < 16; ++r) sm[r] = vis(r) ? sd[r] : sf[r];
        }
        float m;
        if (tg > 0) {
            m = max3(sm[0], sm[1], sm[2]);
#pragma unroll
            for (int r = 3; r < 15; r += 2) m = max3(m, sm[r], sm[r + 1]);
            float m2 = sm[15];
#pragma unroll
            for (int j = 1; j < 8; ++j) {
                const f32x16 s = MFMA32(kf[j], qf, zero);
#pragma unroll
                for (int r = 0; r < 16; r += 4) { m = max3(m, s[r], s[r + 1]); m2 = max3(m2, s[r + 2], s[r + 3]); }
            }
            m = fmaxf(m, m2);
        } else {
            m = -INFINITY;
#pragma unroll
            for (int r = 0; r < 16; ++r) m = fmaxf(m, vis(r) ? sm[r] : -INFINITY);
            for (int j = 8 - wave; j < 8; ++j) {
                const f32x16 s = MFMA32(lds_b128(kaddr + j * 1024), qf, zero);
#pragma unroll
                for (int r = 0; r < 16; r += 2) m = max3(m, s[r], s[r + 1]);
            }
        }
        {
            const auto sw = __builtin_amdgcn_permlane32_swap(__float_as_uint(m), __float_as_uint(m), false, false);
            m = fmaxf(__uint_as_float(sw[0]), __uint_as_float(sw[1]));
        }
        PIN(m);
        TS(11);
        wait1<0>(fv);
        gstore8_dev(ws, WS_F3 + c.myflag, tagv);
        poll_flags(ws, fe_off, fv, tagv);
        issueA(0); issueA(1); issueA(2);
        TS(12);
        f32x16 negm;
#pragma unroll
        for (int r = 0; r < 16; ++r) negm[r] = -m;
        const unsigned vaddr = AT_LDS_V + wave * 2048 + (4 * hh + ((lane & 15) >> 2)) * 64 + ((lane >> 4) & 1) * 32 + (lane & 3) * 8;
        f32x16 o = zero;
        auto pv = [&](const u32x4& p0, const u32x4& p1, int j) {
            const unsigned va = vaddr + j * 2048;
            const bf16x8 v0 = cat8(lds_tr(va), lds_tr(va + 512));
            const bf16x8 v1 = cat8(lds_tr(va + 1024), lds_tr(va + 1536));
            o = MFMA32(v0, __builtin_bit_cast(bf16x8, p0), o);
            o = MFMA32(v1, __builtin_bit_cast(bf16x8, p1), o);
        };
        {
            u32x4 d[2], f[2];
#pragma unroll
            for (int r = 0; r < 16; r += 2) {
                const unsigned pk = cvt_pk_bf16(exp2_clamp(sm[r] - m), exp2_clamp(sm[r + 1] - m));
                const bool c0 = vis(r), c1 = vis(r + 1);
                const unsigned lo = pk & 0xffffu, t0 = c0 ? lo : 0u, dd = c1 ? pk : t0;
                d[r >> 3][(r >> 1) & 3] = dd; f[r >> 3][(r >> 1) & 3] = pk ^ dd;
            }
            pv(d[0], d[1], 8);
            pv(f[0], f[1], 0);
        }
#pragma unroll
        for (int j = 1; j < 8; ++j) {
            const f32x16 s = MFMA32(kf[j], qf, negm);
            u32x4 p0, p1;
#pragma unroll
            for (int r = 0; r < 4; ++r) {
                p0[r] = cvt_pk_bf16(exp2_clamp(s[2 * r]), exp2_clamp(s[2 * r + 1]));
                p1[r] = cvt_pk_bf16(exp2_clamp(s[8 + 2 * r]), exp2_clamp(s[8 + 2 * r + 1]));
            }
            pv(p0, p1, j);
        }
        const float rl = __builtin_amdgcn_rcpf(o[8]);
        const u32x4 pk = {cvt_pk_bf16(o[0] * rl, o[1] * rl), cvt_pk_bf16(o[2] * rl, o[3] * rl), cvt_pk_bf16(o[4] * rl, o[5] * rl), cvt_pk_bf16(o[6] * rl, o[7] * rl)};
        TS(13);
        gstore16_dev(ws + WS_A2 + (size_t)(qt * KSTEPS2 + head) * 1024, lane * 16, pk);
    }
    {
        const char* wd = c.wd;
        const char* wo = (const char*)wo_ + (size_t)wave * 64 * 1024 + tn2 * 128;
        const unsigned vsrc = c.vsrc2, w4k = c.wave4k;
        const int gq = lane >> 4, nb = gq & 1, hh = gq >> 1, r = (lane & 15) >> 2, p = lane & 3;
        const unsigned rdb = w4k + (unsigned)(r * 128 + (((r >> 1) & 1) << 6) + nb * 32 + p * 8);
        const unsigned rdh = rdb + hh * 1024, rdo = rdb + hh * 512;
        f32x16 ac[2][2] = {};
        auto slot_of = [](int P) { const int s = P % 5; return s == 0 ? G2_S0 : s == 1 ? G2_S1 : s == 2 ? G2_S2 : s == 3 ? 0 : 65536; };
        auto issueW = [&](int P) {
            const char* src = P < 7 ? spin(wd) + unit_of(P) * 65536 : spin(wo) + (P - 7) * 32768;
            unsigned l = w4k; asm volatile("" : "+s"(l)); l += slot_of(P);
            glds16(src, vsrc, l); glds16(src + 8192, vsrc, l + 1024); glds16(src + 16384, vsrc, l + 2048); glds16(src + 24576, vsrc, l + 3072);
        };
        auto mma = [&](int P) {
            const unsigned r0 = (P < 7 ? rdh : rdo) + slot_of(P), r1 = r0 ^ 64, t2 = P < 7 ? 512 : 1024;
#pragma unroll
            for (int e = 0; e < 2; ++e) {
                const bf16x8 w0 = cat8(lds_tr(r0 + e * 2048), lds_tr(r0 + e * 2048 + t2));
                const bf16x8 w1 = cat8(lds_tr(r1 + e * 2048), lds_tr(r1 + e * 2048 + t2));
                ac[0][0] = MFMA32(w0, ta[P][2 * e], ac[0][0]); ac[0][1] = MFMA32(w0, ta[P][2 * e + 1], ac[0][1]);
                ac[1][0] = MFMA32(w1, ta[P][2 * e], ac[1][0]); ac[1][1] = MFMA32(w1, ta[P][2 * e + 1], ac[1][1]);
            }
            asm volatile("s_waitcnt lgkmcnt(0)" : "+v"(ac[0][0]), "+v"(ac[0][1]), "+v"(ac[1][0]), "+v"(ac[1][1]) :: "memory"); SBAR();
        };
#define WT(P, N) wait4<N>(ta[P][0], ta[P][1], ta[P][2], ta[P][3])
        u32x2 fv; gload8_dev(fv, ws, fl_off);
        WT(0, 2); WT(1, 2); WT(2, 2);
        mma(0); issueW(5);
        wait1<4>(fv);
        gstore8_dev(ws, WS_FO + (qt * 32 + head) * 8, tagv);
        TS(14);
        poll_flags(ws, fl_off, fv, tagv);
        TS(15);
        issueA(3); issueA(4);
        LGKM_BAR();
        TS(16);
        issueW(3); issueW(4); issueA(5); issueA(6);
        mma(1); issueW(6);
        mma(2); issueW(7);
        gload8_dev(fv, ws, fo_off);
        WT(3, 21); mma(3); issueW(8);
        WT(4, 21); mma(4);
        WT(5, 17); mma(5);
        TS(17);
        wait1<4>(fv); poll_flags(ws, fo_off, fv, tagv);
        TS(18);
        issueA(7); issueA(8);
        WT(6, 12); mma(6);
        WT(7, 4); mma(7);
        WT(8, 0); mma(8);
        TS(19);
        const int tok = tid >> 3, c0 = (tid & 7) * 2;
        const unsigned off = (unsigned)(((tm2 * 64 + tok) * DM + tn2 * 64 + (tid & 7) * 8) * 2);
        u32x4 xv; gload16<0>(xv, x_, off);
        LGKM_BAR();
        TS(20);
        {
            const int t = lane & 31, h2 = lane >> 5;
#pragma unroll
            for (int ct = 0; ct < 2; ++ct)
#pragma unroll
                for (int tt = 0; tt < 2; ++tt)
#pragma unroll
                    for (int g4 = 0; g4 < 4; ++g4) {
                        const int tk = tt * 32 + t, chunk = ct * 8 + g4 * 2 + h2;
                        f32x4 v = {ac[ct][tt][4 * g4], ac[ct][tt][4 * g4 + 1], ac[ct][tt][4 * g4 + 2], ac[ct][tt][4 * g4 + 3]};
                        *(LAS f32x4*)(uintptr_t)(wave * 16384 + tk * 256 + ((chunk ^ (tk & 15)) * 16)) = v;
                    }
        }
        LGKM_BAR();
        TS(21);
        if (b == 0 && tid == 0) gstore8_dev(ws, WS_EPOCH, tagv);
        f32x4 s0 = {0.f, 0.f, 0.f, 0.f}, s1 = s0;
#pragma unroll
        for (int w = 0; w < 8; ++w) {
            s0 += *(const LAS f32x4*)(uintptr_t)(w * 16384 + tok * 256 + ((c0 ^ (tok & 15)) * 16));
            s1 += *(const LAS f32x4*)(uintptr_t)(w * 16384 + tok * 256 + (((c0 + 1) ^ (tok & 15)) * 16));
        }
        wait1<0>(xv);
        float xr[8];
#pragma unroll
        for (int i = 0; i < 4; ++i) { xr[2 * i] = __uint_as_float(xv[i] << 16); xr[2 * i + 1] = __uint_as_float(xv[i] & 0xffff0000u); }
        u32x4 o = {cvt_pk_bf16(s0[0] + xr[0], s0[1] + xr[1]), cvt_pk_bf16(s0[2] + xr[2], s0[3] + xr[3]),
                   cvt_pk_bf16(s1[0] + xr[4], s1[1] + xr[5]), cvt_pk_bf16(s1[2] + xr[6], s1[3] + xr[7])};
        *(u32x4*)((char*)out_ + off) = o;
    }
    TSOUT(0, 22);
}

extern "C" void kernel_launch(void* const* d_in, const int* in_sizes, int n_in, void* d_out, int out_size, void* d_ws, size_t ws_size,
                              hipStream_t stream) {
    const __hip_bfloat16* x = (const __hip_bfloat16*)d_in[0];
    const __hip_bfloat16* wqkv = (const __hip_bfloat16*)d_in[1];
    const __hip_bfloat16* wo = (const __hip_bfloat16*)d_in[2];
    const __hip_bfloat16* wg = (const __hip_bfloat16*)d_in[4];
    const __hip_bfloat16* wu = (const __hip_bfloat16*)d_in[5];
    const __hip_bfloat16* wd = (const __hip_bfloat16*)d_in[6];
    (void)hipFuncSetAttribute((const void*)k_fused, hipFuncAttributeMaxDynamicSharedMemorySize, LDS_BYTES);
    k_fused<<<256, 512, LDS_BYTES, stream>>>(x, wqkv, wo, wg, wu, wd, (char*)d_ws, (__hip_bfloat16*)d_out);
}
```

```cpp
#define PROBE 3
#include <hip/hip_runtime.h>
#include <hip/hip_bf16.h>
#include <stdint.h>

typedef short bf16x8 __attribute__((ext_vector_type(8)));
typedef short s16x4 __attribute__((ext_vector_type(4)));
typedef float f32x2 __attribute__((ext_vector_type(2)));
typedef float f32x4 __attribute__((ext_vector_type(4)));
typedef float f32x16 __attribute__((ext_vector_type(16)));
typedef unsigned u32x4 __attribute__((ext_vector_type(4)));
typedef unsigned u32x2 __attribute__((ext_vector_type(2)));
typedef __bf16 bf16x2_t __attribute__((ext_vector_type(2)));
typedef unsigned long long u64;
#define LAS __attribute__((address_space(3)))
#define DI __device__ __forceinline__

constexpr int SEQ = 2048, DM = 512;
constexpr int KSTEPS2 = 144;
constexpr unsigned WS_EPOCH = 0;
constexpr unsigned WS_F1 = 4096;
constexpr unsigned WS_F2 = WS_F1 + 16384;
constexpr unsigned WS_F3 = WS_F2 + 16384;
constexpr unsigned WS_FO = WS_F3 + 16384;
constexpr unsigned WS_QKV = 131072;
constexpr unsigned WS_A2  = WS_QKV + 6291456;
constexpr unsigned WS_DUMMY = WS_A2 + 9437184;
constexpr unsigned SPIN_CAP = 1u << 14;

DI unsigned cvt_pk_bf16(float lo, float hi) {
    f32x2 v = {lo, hi};
    bf16x2_t r = __builtin_convertvector(v, bf16x2_t);
    return __builtin_bit_cast(unsigned, r);
}
DI float dot2_bf16(unsigned a, unsigned b, float c) {
    return __builtin_amdgcn_fdot2_f32_bf16(__builtin_bit_cast(bf16x2_t, a), __builtin_bit_cast(bf16x2_t, b), c, false);
}
DI void glds16(const void* sbase, unsigned voff, unsigned ldsaddr) {
    asm volatile("s_mov_b32 m0, %2\n\ts_nop 0\n\tglobal_load_lds_dwordx4 %0, %1"
                 :: "v"(voff), "s"(sbase), "s"(ldsaddr) : "memory");
}
template <int IMM> DI void glds16i(const void* sbase, unsigned voff, unsigned ldsbase) {
    asm volatile("" : "+s"(ldsbase));
    glds16(sbase, voff, ldsbase + IMM);
}
DI const char* spin(const char* p) { asm volatile("" : "+s"(p)); return p; }
template <int OFF, class T> DI void gload16(T& dst, const void* sbase, unsigned voff) {
    asm volatile("global_load_dwordx4 %0, %1, %2 offset:%3" : "=v"(dst) : "v"(voff), "s"(sbase), "n"(OFF) : "memory");
}
template <int OFF, class T> DI void gload16_dev(T& dst, const void* sbase, unsigned voff) {
    asm volatile("global_load_dwordx4 %0, %1, %2 offset:%3 sc1" : "=v"(dst) : "v"(voff), "s"(sbase), "n"(OFF) : "memory");
}
DI void gload8_dev(u32x2& dst, const void* sbase, unsigned voff) {
    asm volatile("global_load_dwordx2 %0, %1, %2 sc1" : "=v"(dst) : "v"(voff), "s"(sbase) : "memory");
}
DI void gstore16_dev(const void* sbase, unsigned voff, u32x4 v) {
    asm volatile("global_store_dwordx4 %0, %1, %2 sc1\n\ts_nop 1" :: "v"(voff), "v"(v), "s"(sbase) : "memory");
}
DI void gstore8_dev(const void* sbase, unsigned voff, u32x2 v) {
    asm volatile("global_store_dwordx2 %0, %1, %2 sc1" :: "v"(voff), "v"(v), "s"(sbase) : "memory");
}
template <int N, class T> DI void wait4(T& a, T& b, T& c, T& d) {
    asm volatile("s_waitcnt vmcnt(%4)" : "+v"(a), "+v"(b), "+v"(c), "+v"(d) : "n"(N) : "memory");
}
template <int N, class T> DI void wait1(T& a) { asm volatile("s_waitcnt vmcnt(%1)" : "+v"(a) : "n"(N) : "memory"); }
#define WAITV(N) asm volatile("s_waitcnt vmcnt(" #N ")" ::: "memory")
DI bf16x8 lds_b128(unsigned addr) { return *(const LAS bf16x8*)(uintptr_t)addr; }
DI void lds_st128(unsigned addr, u32x4 v) { *(LAS u32x4*)(uintptr_t)addr = v; }
DI s16x4 lds_tr(unsigned addr) { return __builtin_amdgcn_ds_read_tr16_b64_v4i16((LAS s16x4*)(uintptr_t)addr); }
DI bf16x8 cat8(s16x4 a, s16x4 b) { return __builtin_shufflevector(a, b, 0, 1, 2, 3, 4, 5, 6, 7); }
template <int N> DI void waitv_bar() { asm volatile("s_waitcnt vmcnt(%0) lgkmcnt(0)\n\ts_barrier" :: "n"(N) : "memory"); }
#define LGKM_BAR() asm volatile("s_waitcnt lgkmcnt(0)\n\ts_barrier" ::: "memory")
#define MFMA16(a, b, c) __builtin_amdgcn_mfma_f32_16x16x32_bf16(a, b, c, 0, 0, 0)
#define MFMA32(a, b, c) __builtin_amdgcn_mfma_f32_32x32x16_bf16(a, b, c, 0, 0, 0)
#define SBAR() __builtin_amdgcn_sched_barrier(0)
#define PIN(x) asm volatile("" : "+v"(x))

#define TS(i) do{ if ((i) == 0 || (i) == PROBE) { __builtin_amdgcn_sched_barrier(0); unsigned long long rt_; asm volatile("s_memrealtime %0\n\ts_waitcnt lgkmcnt(0)" : "=s"(rt_) :: "memory"); ts[(i) == 0 ? 0 : 1] = rt_; __builtin_amdgcn_sched_barrier(0);} }while(0)
#define TSDECL unsigned long long ts[2];
#define TSOUT(kid, n) do{ asm volatile("s_waitcnt vmcnt(0)" ::: "memory"); TS(n); unsigned long long d_ = (ts[1] - ts[0]) * 4; if (d_ > 40000) d_ = 40000; unsigned long long e_ = __builtin_amdgcn_s_memrealtime(); while (__builtin_amdgcn_s_memrealtime() - e_ < d_) __builtin_amdgcn_s_sleep(8); }while(0)

constexpr int LX = 0, XSLOT = 16384, LW = 65536, WSL = 8192;
constexpr int AT_LDS_V = 0, AT_LDS_K = 65536;
constexpr int G2_S0 = 98304, G2_S1 = 131072, G2_S2 = 32768;
constexpr int LDS_BYTES = 163840;

constexpr float QSCALE = 0.25f * 1.4426950408889634f;
constexpr float GC1 = -2.f * 0.7978845608028654f * 1.4426950408889634f, GC2 = GC1 * 0.044715f;

struct G1 {
    const char* sb[5];
    unsigned v0, vM, vF;
    unsigned stepM;
    unsigned wave1k, wave4k;
    const char* wd;
    unsigned vsrc2;
    unsigned xa[2], wa[2], xw;
    u32x4 xr[2][8];
    float rstd[2], nmr[2];
    f32x4 acc[5][4];
    float t0, t1, p0, p1;
    unsigned hp[4];
    const char* hdst;
    const char* ws; unsigned myflag, fq_off; u32x2 tagv, pq; int lane;
};
struct Frag { bf16x8 x[4]; s16x4 w[3][2]; };

template <int S> DI void g1_issue(G1& c) {
    constexpr unsigned dst = LW + (S % 12) * WSL;
    if constexpr (S < 24) {
        constexpr int u = S % 3;
        glds16i<dst>(c.sb[u], u == 0 ? c.v0 : c.vM, c.wave1k);
        if constexpr (u == 0) { c.v0 += 64 * 3072u; PIN(c.v0); }
        if constexpr (u == 2) { c.vM += c.stepM; PIN(c.vM); }
    } else {
        constexpr int n = (S - 24) >> 1, u = (S - 24) & 1;
        glds16i<dst>(c.sb[3 + u], c.vF, c.wave1k);
        if constexpr (u == 1) { c.vF += (n & 1) ? (unsigned)(-3 * 64 * 3584) : 64 * 3584u; PIN(c.vF); }
    }
}
template <int S, int e, int T> DI s16x4 g1_wrd(const G1& c) {
    constexpr unsigned off = (S % 12) * WSL + e * 4096 + T * 512;
    return lds_tr(c.wa[off >> 16] + (off & 0xffff));
}
template <int I, int e, int R> DI void p1_rd(Frag& f, const G1& c) {
    if constexpr (R < 2) f.w[0][R] = g1_wrd<3 * I, e, R>(c);
    else if constexpr (R < 6) f.x[R - 2] = lds_b128(c.xa[e] + LX + (I & 3) * XSLOT + (R - 2) * 2048);
    else f.w[(R - 4) >> 1][R & 1] = g1_wrd<3 * I + ((R - 4) >> 1), e, (R & 1)>(c);
}
template <int N, int R> DI void p2_rd(Frag& f, const G1& c) {
    constexpr int n = N >> 1, e = N & 1, slice = 6 - 2 * (n >> 1) + (n & 1);
    if constexpr (R < 2) f.w[0][R] = g1_wrd<24 + 2 * n, e, R>(c);
    else if constexpr (R < 6) f.x[R - 2] = lds_b128(c.xa[e] + LX + (slice & 3) * XSLOT + (R - 2) * 2048);
    else f.w[1][R & 1] = g1_wrd<25 + 2 * n, e, (R & 1)>(c);
}
template <int J0, int M> DI void g1_mm(G1& c, const Frag& f) {
    constexpr int j = M >> 2, i = M & 3;
    c.acc[J0 + j][i] = MFMA16(cat8(f.w[j][0], f.w[j][1]), f.x[i], c.acc[J0 + j][i]);
    PIN(c.acc[J0 + j][i]);
}
template <int I, int row> DI void g1_xwrite(G1& c) { lds_st128((row == 0 ? c.xw : (c.xw ^ 64) + 1024) + LX + (I & 3) * XSLOT, c.xr[row][I]); }
template <int I, int OP> DI void g1_normop(G1& c) {
    constexpr int u = OP / 5, o = OP % 5, row = u >> 2, r = u & 3;
    if constexpr (o == 0) { c.t0 = __uint_as_float(c.xr[row][I][r] << 16); PIN(c.t0); }
    if constexpr (o == 1) { c.t1 = __uint_as_float(c.xr[row][I][r] & 0xffff0000u); PIN(c.t1); }
    if constexpr (o == 2) { c.t0 = __builtin_fmaf(c.t0, c.rstd[row], c.nmr[row]); asm volatile("" : "+v"(c.t0), "+v"(c.t1)); }
    if constexpr (o == 3) { c.t1 = __builtin_fmaf(c.t1, c.rstd[row], c.nmr[row]); PIN(c.t1); }
    if constexpr (o == 4) {
        unsigned w = cvt_pk_bf16(c.t0, c.t1); PIN(w);
        c.xr[row][I][r] = w;
        if constexpr (r == 3) g1_xwrite<I, row>(c);
    }
}
template <int JG, int OP> DI void g1_geluop(G1& c, const char* dst) {
    constexpr int pr = OP / 18, o = OP % 18, i = pr >> 1, r = (pr & 1) * 2;
    const float g0 = c.acc[JG][i][r], g1 = c.acc[JG][i][r + 1];
#define P2 asm volatile("" : "+v"(c.t0), "+v"(c.t1))
    if constexpr (o == 0) { c.t0 = g0 * g0; PIN(c.t0); }
    if constexpr (o == 1) { c.t1 = g1 * g1; P2; }
    if constexpr (o == 2) { c.t0 = __builtin_fmaf(c.t0, GC2, GC1); P2; }
    if constexpr (o == 3) { c.t1 = __builtin_fmaf(c.t1, GC2, GC1); P2; }
    if constexpr (o == 4) { c.t0 = c.t0 * g0; P2; }
    if constexpr (o == 5) { c.t1 = c.t1 * g1; P2; }
    if constexpr (o == 6) { c.t0 = __builtin_amdgcn_exp2f(c.t0); P2; }
    if constexpr (o == 7) { c.p0 = g0 * c.acc[JG + 1][i][r]; PIN(c.p0); }
    if constexpr (o == 8) { c.t1 = __builtin_amdgcn_exp2f(c.t1); P2; }
    if constexpr (o == 9) { c.p1 = g1 * c.acc[JG + 1][i][r + 1]; asm volatile("" : "+v"(c.p0), "+v"(c.p1)); }
    if constexpr (o == 10) { c.t0 = 1.f + c.t0; P2; }
    if constexpr (o == 11) { c.t1 = 1.f + c.t1; P2; }
    if constexpr (o == 12) { c.t0 = __builtin_amdgcn_rcpf(c.t0); P2; }
    if constexpr (o == 13) { c.t1 = __builtin_amdgcn_rcpf(c.t1); P2; }
    if constexpr (o == 14) { c.p0 = c.p0 * c.t0; asm volatile("" : "+v"(c.p0), "+v"(c.p1)); }
    if constexpr (o == 15) { c.p1 = c.p1 * c.t1; PIN(c.p1); }
    if constexpr (o == 16) { c.hp[pr & 3] = cvt_pk_bf16(c.p0, c.p1); PIN(c.hp[pr & 3]); }
    if constexpr (o == 17 && (pr & 3) == 3) {
        const auto s0 = __builtin_amdgcn_permlane16_swap(c.hp[0], c.hp[2], false, false);
        const auto s1 = __builtin_amdgcn_permlane16_swap(c.hp[1], c.hp[3], false, false);
        gstore16_dev(dst, (unsigned)(c.lane * 16 + (pr >> 2) * (KSTEPS2 * 1024)), u32x4{s0[0], s1[0], s0[1], s1[1]});
    }
#undef P2
}
template <int I, int G> DI void p1_gap(G1& c, Frag& f0, Frag& f1) {
    constexpr int h = G / 12, k = G % 12;
    Frag& fm = h == 0 ? f1 : f0;
    Frag& fr = h == 0 ? f0 : f1;
    if constexpr (G % 6 == 0) __builtin_amdgcn_s_setprio(3 - G / 6);
    if constexpr (!(I == 0 && h == 0)) g1_mm<0, k>(c, fm);
    SBAR();
    if constexpr (k < 10) p1_rd<I, h, k>(fr, c);
    if constexpr (I + 1 < 8 && G < 20) { g1_normop<I + 1, 2 * G>(c); g1_normop<I + 1, 2 * G + 1>(c); }
    if constexpr ((G % 8) == 7) g1_issue<3 * I + 9 + G / 8>(c);
    SBAR();
}
template <int I> DI void p1_interval(G1& c, Frag& f0, Frag& f1) {
#define CH(C) p1_gap<I, C>(c, f0, f1)
#define CH6(B) CH(B); CH(B + 1); CH(B + 2); CH(B + 3); CH(B + 4); CH(B + 5);
    CH6(0) CH6(6) CH6(12) CH6(18)
#undef CH
#undef CH6
    waitv_bar<(I < 7 ? 6 : 5)>();
}
template <int J, int G> DI void p2_gap(G1& c, Frag& f0, Frag& f1) {
    constexpr int qd = G / 8, k = G % 8, N = 4 * J + qd;
    Frag& fm = (qd & 1) == 0 ? f1 : f0;
    Frag& fr = (qd & 1) == 0 ? f0 : f1;
    if constexpr (k == 0) __builtin_amdgcn_s_setprio(3 - qd);
    if constexpr (N > 0) g1_mm<3, k>(c, fm);
    SBAR();
    if constexpr (N > 0) p2_rd<N, k>(fr, c);
    constexpr int gg = 32 * J + G - 8;
    if constexpr (gg >= 0 && gg < 72) { g1_geluop<1, 2 * gg>(c, c.hdst); g1_geluop<1, 2 * gg + 1>(c, c.hdst); }
    if constexpr (J == 1 && (G % 8) == 3) g1_issue<36 + G / 8>(c);
    if constexpr ((J == 1 || J == 2) && (G % 8) == 6) g1_xwrite<(J == 1 ? 2 : 0) + (G >> 4), (G >> 3) & 1>(c);
    if constexpr (J == 2 && G == 0) gstore8_dev(c.ws, WS_F1 + c.myflag, c.tagv);
    if constexpr (J == 2 && G >= 20 && (G % 3) == 2) glds16i<G2_S0 + ((G - 20) / 3) * 1024>(spin(c.wd) + ((G - 20) / 3) * 8192, c.vsrc2, c.wave4k);
    if constexpr (J == 3 && G == 8) gload8_dev(c.pq, c.ws, c.fq_off);
    if constexpr (J == 3 && G >= 9 && G < 31 && (G % 3) == 0) {
        constexpr int n = (G - 9) / 3;
        glds16i<(n < 4 ? G2_S1 : G2_S2) + (n & 3) * 1024>(spin(c.wd) + (n < 4 ? 131072 : 262144) + (n & 3) * 8192, c.vsrc2, c.wave4k);
    }
    SBAR();
}
template <int J, int G0> DI void p2_interval(G1& c, Frag& f0, Frag& f1) {
#define CH(C) p2_gap<J, C>(c, f0, f1)
#define CH8(B) CH(B); CH(B + 1); CH(B + 2); CH(B + 3); CH(B + 4); CH(B + 5); CH(B + 6); CH(B + 7);
    if constexpr (G0 == 0) { CH8(0) }
    CH8(8) CH8(16) CH8(24)
#undef CH
#undef CH8
}
DI float dpp_sum8(float v) {
    v += __builtin_bit_cast(float, __builtin_amdgcn_update_dpp(0, __builtin_bit_cast(int, v), 0xB1, 0xf, 0xf, true));
    v += __builtin_bit_cast(float, __builtin_amdgcn_update_dpp(0, __builtin_bit_cast(int, v), 0x4E, 0xf, 0xf, true));
    v += __builtin_bit_cast(float, __builtin_amdgcn_update_dpp(0, __builtin_bit_cast(int, v), 0x141, 0xf, 0xf, true));
    return v;
}
DI float exp2_clamp(float v) { return __builtin_amdgcn_fmed3f(__builtin_amdgcn_exp2f(v), 0.f, 1.f); }
DI float max3(float a, float b, float c) { return __builtin_fmaxf(__builtin_fmaxf(a, b), c); }
DI bool vis(int R) {
    const int bb = (R & 3) + 8 * (R >> 2);
    const u64 lo = 0xffffffffull & (0xffffffffull << bb), hi = 0xffffffffull & (0xffffffffull << (bb + 4));
    return __builtin_amdgcn_inverse_ballot_w64(lo | (hi << 32));
}
DI void poll_flags(const char* ws, unsigned foff, u32x2 v, u32x2 tag) {
#pragma clang loop unroll(disable)
    for (unsigned spins = 0; spins < SPIN_CAP; ++spins) {
        if (__all(v[0] == tag[0] && v[1] == tag[1])) break;
        __builtin_amdgcn_s_sleep(2);
        gload8_dev(v, ws, foff); wait1<0>(v);
    }
}

__global__ __launch_bounds__(512, 2) void k_fused(const __hip_bfloat16* __restrict__ x_, const __hip_bfloat16* __restrict__ wqkv_,
        const __hip_bfloat16* __restrict__ wo_, const __hip_bfloat16* __restrict__ wg_, const __hip_bfloat16* __restrict__ wu_,
        const __hip_bfloat16* __restrict__ wd_, char* __restrict__ ws, __hip_bfloat16* __restrict__ out_) {
    const int b = blockIdx.x, xcd = b & 7, idx = b >> 3;
    TSDECL TS(0);
    const int tid = threadIdx.x, lane = tid & 63;
    const int wave = __builtin_amdgcn_readfirstlane(tid >> 6);
    const int tm2 = xcd * 4 + (idx >> 3), tn2 = idx & 7;
    const int head = xcd * 4 + (idx & 3), tg = idx >> 2, qt = 8 * tg + wave;
    const int tm = (xcd >> 2) * 8 + (idx >> 2), cq = xcd & 3, cp = idx & 3, tn = cq * 4 + cp;
    const int wr = wave >> 2, wc = wave & 3;
    const bool typeA = cp < 3;
    const int qu0 = 6 * cq + cp, f1 = 7 * cq + 2 * cp, f2 = f1 + (typeA ? 1 : 0);
    const int tok0 = tm * 128 + 64 * wr;
    G1 c; c.wave1k = wave * 1024; c.wave4k = wave * 4096; c.ws = ws; c.lane = lane;
    u32x2 ep; gload8_dev(ep, ws, WS_EPOCH);
    {
        const char* xp = (const char*)x_ + ((size_t)tm * 128 + 16 * wave) * 1024;
        const unsigned v0 = (lane >> 3) * 1024 + (lane & 7) * 16, v1 = v0 + 8192;
        gload16<0>(c.xr[0][0], xp, v0);   gload16<0>(c.xr[1][0], xp, v1);   gload16<128>(c.xr[0][1], xp, v0); gload16<128>(c.xr[1][1], xp, v1);
        gload16<256>(c.xr[0][2], xp, v0); gload16<256>(c.xr[1][2], xp, v1); gload16<384>(c.xr[0][3], xp, v0); gload16<384>(c.xr[1][3], xp, v1);
        gload16<512>(c.xr[0][4], xp, v0); gload16<512>(c.xr[1][4], xp, v1); gload16<640>(c.xr[0][5], xp, v0); gload16<640>(c.xr[1][5], xp, v1);
        gload16<768>(c.xr[0][6], xp, v0); gload16<768>(c.xr[1][6], xp, v1); gload16<896>(c.xr[0][7], xp, v0); gload16<896>(c.xr[1][7], xp, v1);
    }
    {
        const char* wqkv = (const char*)wqkv_; const char* wg = (const char*)wg_; const char* wu = (const char*)wu_;
        const unsigned strM = typeA ? 3584u : 3072u;
        const int r = lane >> 3, ch = lane & 7;
        const int g = ((r >> 1) & 1) | ((wave & 1) << 1);
        const unsigned colw = (unsigned)((ch ^ (g << 1)) * 16);
        c.v0 = (8 * wave + r) * 3072u + colw; c.vM = (8 * wave + r) * strM + colw; c.vF = (384 + 8 * wave + r) * 3584u + colw;
        c.stepM = 64 * strM;
        c.sb[0] = wqkv + qu0 * 128;
        c.sb[1] = typeA ? wg + f1 * 128 : c.sb[0] + 128;
        c.sb[2] = typeA ? wu + f1 * 128 : c.sb[0] + 256;
        c.sb[3] = wg + f2 * 128;
        c.sb[4] = wu + f2 * 128;
        c.wd = (const char*)wd_ + (size_t)((wave >> 1) * 448 + (wave & 1) * 32) * 1024 + tn2 * 128;
        c.vsrc2 = (unsigned)((lane >> 3) * 1024 + (((lane & 7) ^ (((lane >> 4) & 1) << 2)) * 16));
        c.hdst = typeA ? ws + WS_A2 + (size_t)((tok0 >> 5) * KSTEPS2 + 32 + 4 * f1 + wc) * 1024 : ws + WS_DUMMY + ((tm * 4 + cq) * 8 + wave) * 1024;
        c.myflag = (unsigned)(((tm * 16 + tn) * 8 + wave) * 8);
    }
    {
        const int t = lane & 15, q = lane >> 4;
        c.xa[0] = (unsigned)((64 * wr + t) * 128 + ((q ^ (t >> 1)) * 16));
        c.xa[1] = c.xa[0] ^ 64;
        const int r = (lane & 15) >> 2, p = lane & 3;
        const int g = ((r >> 1) & 1) | ((q & 1) << 1);
        c.wa[0] = (unsigned)(LW + (8 * q + r) * 128 + (p >> 1) * 16 + (p & 1) * 8 + ((wc ^ g) << 5));
        c.wa[1] = c.wa[0] + 65536; PIN(c.wa[0]); PIN(c.wa[1]);
        const int row = 16 * wave + (lane >> 3);
        c.xw = (unsigned)(row * 128 + (((lane & 7) ^ ((row >> 1) & 7)) * 16));
        const int sel = lane < 5 ? lane : 0;
        const int which = sel == 0 ? 0 : 2 - (sel & 1);
        const int T = (sel >= 3 && tg > 0) ? qt - 8 : qt;
        const int u = which * 8 + (head >> 2);
        const int pq = (u * 43) >> 8, pr = u - 6 * pq;
        c.fq_off = WS_F1 + (((T >> 2) * 16 + 4 * pq + (pr < 3 ? pr : 3)) * 8 + ((T >> 1) & 1) * 4 + (head & 3)) * 8;
    }
    TS(1);
    {
        const unsigned ones = 0x3f803f80u;
        float s1[2][2] = {}, s2[2][2] = {};
        auto stat = [&](int s) {
#pragma unroll
            for (int h = 0; h < 2; ++h)
#pragma unroll
                for (int r = 0; r < 4; ++r) {
                    s1[h][r & 1] = dot2_bf16(c.xr[h][s][r], ones, s1[h][r & 1]);
                    s2[h][r & 1] = dot2_bf16(c.xr[h][s][r], c.xr[h][s][r], s2[h][r & 1]);
                }
            asm volatile("" : "+v"(s1[0][0]), "+v"(s1[0][1]), "+v"(s1[1][0]), "+v"(s1[1][1]), "+v"(s2[0][0]), "+v"(s2[0][1]), "+v"(s2[1][0]), "+v"(s2[1][1]));
        };
        wait4<12>(c.xr[0][0], c.xr[1][0], c.xr[0][1], c.xr[1][1]); PIN(ep); stat(0); stat(1);
        wait4<8>(c.xr[0][2], c.xr[1][2], c.xr[0][3], c.xr[1][3]);  stat(2); stat(3);
        g1_issue<0>(c); g1_issue<1>(c); g1_issue<2>(c);
        wait4<7>(c.xr[0][4], c.xr[1][4], c.xr[0][5], c.xr[1][5]);  stat(4); stat(5);
        wait4<3>(c.xr[0][6], c.xr[1][6], c.xr[0][7], c.xr[1][7]);  stat(6); stat(7);
        g1_issue<3>(c); g1_issue<4>(c); g1_issue<5>(c);
#pragma unroll
        for (int h = 0; h < 2; ++h) {
            const float t1 = dpp_sum8(s1[h][0] + s1[h][1]), t2 = dpp_sum8(s2[h][0] + s2[h][1]);
            const float mean = t1 * (1.f / 512.f);
            const float var = fmaxf(t2 * (1.f / 512.f) - mean * mean, 0.f) + 1e-5f;
            c.rstd[h] = __builtin_amdgcn_rsqf(var); c.nmr[h] = -mean * c.rstd[h];
        }
    }
    {
        u64 z = ((u64)(unsigned)__builtin_amdgcn_readfirstlane(ep[1]) << 32 | (unsigned)__builtin_amdgcn_readfirstlane(ep[0])) + 0x9E3779B97F4A7C15ull;
        z = (z ^ (z >> 30)) * 0xBF58476D1CE4E5B9ull; z = (z ^ (z >> 27)) * 0x94D049BB133111EBull; z ^= z >> 31;
        c.tagv = u32x2{(unsigned)z, (unsigned)(z >> 32)};
    }
    TS(2);
#define NO(O) g1_normop<0, O>(c)
#define NO10(B) NO(B); NO(B + 1); NO(B + 2); NO(B + 3); NO(B + 4); NO(B + 5); NO(B + 6); NO(B + 7); NO(B + 8); NO(B + 9);
    NO10(0) NO10(10) NO10(20) NO10(30)
#undef NO
#undef NO10
    g1_issue<6>(c); g1_issue<7>(c); g1_issue<8>(c);
#pragma unroll
    for (int j = 0; j < 5; ++j)
#pragma unroll
        for (int i = 0; i < 4; ++i) c.acc[j][i] = f32x4{0.f, 0.f, 0.f, 0.f};
    Frag f0, f1_;
    waitv_bar<6>();
    TS(3);
    p1_interval<0>(c, f0, f1_); p1_interval<1>(c, f0, f1_); p1_interval<2>(c, f0, f1_); p1_interval<3>(c, f0, f1_);
    TS(4);
    p1_interval<4>(c, f0, f1_); p1_interval<5>(c, f0, f1_); p1_interval<6>(c, f0, f1_); p1_interval<7>(c, f0, f1_);
    TS(5);
    {
        __builtin_amdgcn_s_setprio(3);
#define LG(K) g1_mm<0, K>(c, f1_); SBAR(); if constexpr (K < 8) p2_rd<0, (K < 8 ? K : 0)>(f0, c); if constexpr (K >= 8 && K < 11) g1_issue<33 + (K >= 8 && K < 11 ? K - 8 : 0)>(c); SBAR();
        LG(0) LG(1) LG(2) LG(3) LG(4) LG(5) LG(6) LG(7) LG(8) LG(9) LG(10) LG(11)
#undef LG
        const int t = lane & 15, q = lane >> 4;
        const unsigned qoff = (unsigned)((tok0 + (q & 1) * 16 + t) * 32 + (q >> 1) * 16);
        auto store_qkv = [&](int j) {
            const int hcol = (qu0 + j) * 4 + wc;
            const float sc = hcol < 32 ? QSCALE : 1.f;
            const char* dst = ws + WS_QKV + (size_t)hcol * SEQ * 32;
#pragma unroll
            for (int i = 0; i < 4; i += 2) {
                const unsigned a0 = cvt_pk_bf16(c.acc[j][i][0] * sc, c.acc[j][i][1] * sc), a1 = cvt_pk_bf16(c.acc[j][i][2] * sc, c.acc[j][i][3] * sc);
                const unsigned b0 = cvt_pk_bf16(c.acc[j][i + 1][0] * sc, c.acc[j][i + 1][1] * sc), b1 = cvt_pk_bf16(c.acc[j][i + 1][2] * sc, c.acc[j][i + 1][3] * sc);
                const auto s0 = __builtin_amdgcn_permlane16_swap(a0, b0, false, false);
                const auto s1 = __builtin_amdgcn_permlane16_swap(a1, b1, false, false);
                gstore16_dev(dst, qoff + i * 512, u32x4{s0[0], s1[0], s0[1], s1[1]});
            }
        };
        store_qkv(0);
        if (!typeA) { store_qkv(1); store_qkv(2); }
        PIN(c.xa[0]); PIN(c.xa[1]); PIN(c.wa[0]); PIN(c.wa[1]); PIN(c.xw);
    }
    p2_interval<0, 8>(c, f0, f1_); waitv_bar<6>();
    p2_interval<1, 0>(c, f0, f1_); waitv_bar<5>();
    TS(6);
    p2_interval<2, 0>(c, f0, f1_); waitv_bar<6>();
    p2_interval<3, 0>(c, f0, f1_); LGKM_BAR();
    g1_mm<3, 0>(c, f1_); g1_mm<3, 1>(c, f1_); g1_mm<3, 2>(c, f1_); g1_mm<3, 3>(c, f1_); g1_mm<3, 4>(c, f1_); g1_mm<3, 5>(c, f1_); g1_mm<3, 6>(c, f1_); g1_mm<3, 7>(c, f1_);
    __builtin_amdgcn_s_setprio(0);
    TS(7);
    const u32x2 tagv = c.tagv;
    wait1<8>(c.pq);
    gstore8_dev(ws, WS_F2 + c.myflag, tagv);
    poll_flags(ws, c.fq_off, c.pq, tagv);
    TS(8);
    unsigned fe_off, fl_off, fo_off;
    {
        const int pb = ((tm2 >> 1) * 16 + (wave >> 1) * 4) * 8 + (tm2 & 1) * 4 + (wave & 1) * 2 + (lane & 1);
        const int cpe = (lane >> 1) < 3 ? (lane >> 1) : 0, cpl = (lane >> 1) & 3;
        fe_off = WS_F2 + (pb + cpe * 8) * 8;
        fl_off = WS_F3 + (pb + cpl * 8) * 8;
        if (b == 0 && lane >= 32) fl_off = WS_F1 + (wave * 32 + lane - 32) * 64;
        fo_off = WS_FO + ((2 * tm2 + ((lane >> 2) & 1)) * 32 + 4 * wave + (lane & 3)) * 8;
    }
    const char* a2h = ws + WS_A2 + (size_t)(tm2 * 2 * KSTEPS2 + 32 + 28 * (wave >> 1) + 2 * (wave & 1)) * 1024;
    const char* a2o = ws + WS_A2 + (size_t)(tm2 * 2 * KSTEPS2 + 4 * wave) * 1024;
    const unsigned va2 = lane * 16, vb2 = lane * 16 + KSTEPS2 * 1024;
    bf16x8 ta[9][4];
    auto unit_of = [](int P) { return P < 3 ? 2 * P : (P < 6 ? 2 * (P - 3) + 1 : 6); };
    auto issueA = [&](int P) {
        const char* src = P < 7 ? spin(a2h) + unit_of(P) * 4096 : spin(a2o) + (P - 7) * 2048;
        gload16_dev<0>(ta[P][0], src, va2); gload16_dev<0>(ta[P][1], src, vb2); gload16_dev<1024>(ta[P][2], src, va2); gload16_dev<1024>(ta[P][3], src, vb2);
    };
    {
        const char* Q = ws + WS_QKV + (size_t)head * SEQ * 32;
        const char* K = Q + (size_t)32 * SEQ * 32;
        const char* V = K + (size_t)32 * SEQ * 32;
        const int a = lane & 31, hh = lane >> 5;
        bf16x8 qf;
        {
            const bool lo_ok = tg > 0;
            const int kt0 = lo_ok ? qt - 8 : qt;
            const unsigned vk = a * 32 + hh * 16, vv = lane * 16;
            u32x4 k0, k1, v0, v1;
            gload16_dev<0>(k0, K + kt0 * 1024, vk); gload16_dev<0>(k1, K + qt * 1024, vk);
            gload16_dev<0>(v0, V + kt0 * 1024, vv); gload16_dev<0>(v1, V + qt * 1024, vv);
            gload16_dev<0>(qf, Q + qt * 1024, vk);
            {
                const char* dst = ws + WS_A2 + (size_t)((tok0 >> 5) * KSTEPS2 + 32 + 4 * f2 + wc) * 1024;
                unsigned hp[8];
#pragma unroll
                for (int i = 0; i < 4; ++i)
#pragma unroll
                    for (int r = 0; r < 4; r += 2) {
                        float h[2];
#pragma unroll
                        for (int z = 0; z < 2; ++z) {
                            const float ga = c.acc[3][i][r + z], ua = c.acc[4][i][r + z];
                            const float e = __builtin_amdgcn_exp2f(ga * __builtin_fmaf(ga * ga, GC2, GC1));
                            h[z] = ga * ua * __builtin_amdgcn_rcpf(1.f + e);
                        }
                        hp[2 * i + (r >> 1)] = cvt_pk_bf16(h[0], h[1]);
                    }
#pragma unroll
                for (int i = 0; i < 2; ++i) {
                    const auto s0 = __builtin_amdgcn_permlane16_swap(hp[4 * i], hp[4 * i + 2], false, false);
                    const auto s1 = __builtin_amdgcn_permlane16_swap(hp[4 * i + 1], hp[4 * i + 3], false, false);
                    gstore16_dev(dst, (unsigned)(lane * 16 + i * (KSTEPS2 * 1024)), u32x4{s0[0], s1[0], s0[1], s1[1]});
                }
            }
            TS(9);
            const unsigned one = (lane & 1) ? 0u : 0x3f80u;
            u32x4 c1 = {one, 0u, one, 0u}, c0 = c1;
            if (!lo_ok) c0 = u32x4{0u, 0u, 0u, 0u};
            const unsigned va = AT_LDS_V + wave * 2048 + (lane >> 1) * 64 + (lane & 1) * 16;
            lds_st128(va + 32, c0); lds_st128(va + 16384 + 32, c1);
            wait4<3>(k0, k1, v0, v1);
            if (!lo_ok) v0 = u32x4{0u, 0u, 0u, 0u};
            lds_st128(AT_LDS_K + wave * 1024 + lane * 16, k0); lds_st128(AT_LDS_K + 8192 + wave * 1024 + lane * 16, k1);
            lds_st128(va, v0); lds_st128(va + 16384, v1);
            wait1<2>(qf);
        }
        u32x2 fv; gload8_dev(fv, ws, fe_off);
        LGKM_BAR();
        TS(10);
        const unsigned kaddr = AT_LDS_K + wave * 1024 + lane * 16;
        bf16x8 kf[9];
#pragma unroll
        for (int j = 0; j < 9; ++j) kf[j] = lds_b128(kaddr + j * 1024);
        const f32x16 zero = {};
        f32x16 sm;
        {
            const f32x16 sf = MFMA32(kf[0], qf, zero);
            const f32x16 sd = MFMA32(kf[8], qf, zero);
#pragma unroll
            for (int r = 0; r < 16; ++r) sm[r] = vis(r) ? sd[r] : sf[r];
        }
        float m;
        if (tg > 0) {
            m = max3(sm[0], sm[1], sm[2]);
#pragma unroll
            for (int r = 3; r < 15; r += 2) m = max3(m, sm[r], sm[r + 1]);
            float m2 = sm[15];
#pragma unroll
            for (int j = 1; j < 8; ++j) {
                const f32x16 s = MFMA32(kf[j], qf, zero);
#pragma unroll
                for (int r = 0; r < 16; r += 4) { m = max3(m, s[r], s[r + 1]); m2 = max3(m2, s[r + 2], s[r + 3]); }
            }
            m = fmaxf(m, m2);
        } else {
            m = -INFINITY;
#pragma unroll
            for (int r = 0; r < 16; ++r) m = fmaxf(m, vis(r) ? sm[r] : -INFINITY);
            for (int j = 8 - wave; j < 8; ++j) {
                const f32x16 s = MFMA32(lds_b128(kaddr + j * 1024), qf, zero);
#pragma unroll
                for (int r = 0; r < 16; r += 2) m = max3(m, s[r], s[r + 1]);
            }
        }
        {
            const auto sw = __builtin_amdgcn_permlane32_swap(__float_as_uint(m), __float_as_uint(m), false, false);
            m = fmaxf(__uint_as_float(sw[0]), __uint_as_float(sw[1]));
        }
        PIN(m);
        TS(11);
        wait1<0>(fv);
        gstore8_dev(ws, WS_F3 + c.myflag, tagv);
        poll_flags(ws, fe_off, fv, tagv);
        issueA(0); issueA(1); issueA(2);
        TS(12);
        f32x16 negm;
#pragma unroll
        for (int r = 0; r < 16; ++r) negm[r] = -m;
        const unsigned vaddr = AT_LDS_V + wave * 2048 + (4 * hh + ((lane & 15) >> 2)) * 64 + ((lane >> 4) & 1) * 32 + (lane & 3) * 8;
        f32x16 o = zero;
        auto pv = [&](const u32x4& p0, const u32x4& p1, int j) {
            const unsigned va = vaddr + j * 2048;
            const bf16x8 v0 = cat8(lds_tr(va), lds_tr(va + 512));
            const bf16x8 v1 = cat8(lds_tr(va + 1024), lds_tr(va + 1536));
            o = MFMA32(v0, __builtin_bit_cast(bf16x8, p0), o);
            o = MFMA32(v1, __builtin_bit_cast(bf16x8, p1), o);
        };
        {
            u32x4 d[2], f[2];
#pragma unroll
            for (int r = 0; r < 16; r += 2) {
                const unsigned pk = cvt_pk_bf16(exp2_clamp(sm[r] - m), exp2_clamp(sm[r + 1] - m));
                const bool c0 = vis(r), c1 = vis(r + 1);
                const unsigned lo = pk & 0xffffu, t0 = c0 ? lo : 0u, dd = c1 ? pk : t0;
                d[r >> 3][(r >> 1) & 3] = dd; f[r >> 3][(r >> 1) & 3] = pk ^ dd;
            }
            pv(d[0], d[1], 8);
            pv(f[0], f[1], 0);
        }
#pragma unroll
        for (int j = 1; j < 8; ++j) {
            const f32x16 s = MFMA32(kf[j], qf, negm);
            u32x4 p0, p1;
#pragma unroll
            for (int r = 0; r < 4; ++r) {
                p0[r] = cvt_pk_bf16(exp2_clamp(s[2 * r]), exp2_clamp(s[2 * r + 1]));
                p1[r] = cvt_pk_bf16(exp2_clamp(s[8 + 2 * r]), exp2_clamp(s[8 + 2 * r + 1]));
            }
            pv(p0, p1, j);
        }
        const float rl = __builtin_amdgcn_rcpf(o[8]);
        const u32x4 pk = {cvt_pk_bf16(o[0] * rl, o[1] * rl), cvt_pk_bf16(o[2] * rl, o[3] * rl), cvt_pk_bf16(o[4] * rl, o[5] * rl), cvt_pk_bf16(o[6] * rl, o[7] * rl)};
        TS(13);
        gstore16_dev(ws + WS_A2 + (size_t)(qt * KSTEPS2 + head) * 1024, lane * 16, pk);
    }
    {
        const char* wd = c.wd;
        const char* wo = (const char*)wo_ + (size_t)wave * 64 * 1024 + tn2 * 128;
        const unsigned vsrc = c.vsrc2, w4k = c.wave4k;
        const int gq = lane >> 4, nb = gq & 1, hh = gq >> 1, r = (lane & 15) >> 2, p = lane & 3;
        const unsigned rdb = w4k + (unsigned)(r * 128 + (((r >> 1) & 1) << 6) + nb * 32 + p * 8);
        const unsigned rdh = rdb + hh * 1024, rdo = rdb + hh * 512;
        f32x16 ac[2][2] = {};
        auto slot_of = [](int P) { const int s = P % 5; return s == 0 ? G2_S0 : s == 1 ? G2_S1 : s == 2 ? G2_S2 : s == 3 ? 0 : 65536; };
        auto issueW = [&](int P) {
            const char* src = P < 7 ? spin(wd) + unit_of(P) * 65536 : spin(wo) + (P - 7) * 32768;
            unsigned l = w4k; asm volatile("" : "+s"(l)); l += slot_of(P);
            glds16(src, vsrc, l); glds16(src + 8192, vsrc, l + 1024); glds16(src + 16384, vsrc, l + 2048); glds16(src + 24576, vsrc, l + 3072);
        };
        auto mma = [&](int P) {
            const unsigned r0 = (P < 7 ? rdh : rdo) + slot_of(P), r1 = r0 ^ 64, t2 = P < 7 ? 512 : 1024;
#pragma unroll
            for (int e = 0; e < 2; ++e) {
                const bf16x8 w0 = cat8(lds_tr(r0 + e * 2048), lds_tr(r0 + e * 2048 + t2));
                const bf16x8 w1 = cat8(lds_tr(r1 + e * 2048), lds_tr(r1 + e * 2048 + t2));
                ac[0][0] = MFMA32(w0, ta[P][2 * e], ac[0][0]); ac[0][1] = MFMA32(w0, ta[P][2 * e + 1], ac[0][1]);
                ac[1][0] = MFMA32(w1, ta[P][2 * e], ac[1][0]); ac[1][1] = MFMA32(w1, ta[P][2 * e + 1], ac[1][1]);
            }
            asm volatile("s_waitcnt lgkmcnt(0)" : "+v"(ac[0][0]), "+v"(ac[0][1]), "+v"(ac[1][0]), "+v"(ac[1][1]) :: "memory"); SBAR();
        };
#define WT(P, N) wait4<N>(ta[P][0], ta[P][1], ta[P][2], ta[P][3])
        u32x2 fv; gload8_dev(fv, ws, fl_off);
        WT(0, 2); WT(1, 2); WT(2, 2);
        mma(0); issueW(5);
        wait1<4>(fv);
        gstore8_dev(ws, WS_FO + (qt * 32 + head) * 8, tagv);
        TS(14);
        poll_flags(ws, fl_off, fv, tagv);
        TS(15);
        issueA(3); issueA(4);
        LGKM_BAR();
        TS(16);
        issueW(3); issueW(4); issueA(5); issueA(6);
        mma(1); issueW(6);
        mma(2); issueW(7);
        gload8_dev(fv, ws, fo_off);
        WT(3, 21); mma(3); issueW(8);
        WT(4, 21); mma(4);
        WT(5, 17); mma(5);
        TS(17);
        wait1<4>(fv); poll_flags(ws, fo_off, fv, tagv);
        TS(18);
        issueA(7); issueA(8);
        WT(6, 12); mma(6);
        WT(7, 4); mma(7);
        WT(8, 0); mma(8);
        TS(19);
        const int tok = tid >> 3, c0 = (tid & 7) * 2;
        const unsigned off = (unsigned)(((tm2 * 64 + tok) * DM + tn2 * 64 + (tid & 7) * 8) * 2);
        u32x4 xv; gload16<0>(xv, x_, off);
        LGKM_BAR();
        TS(20);
        {
            const int t = lane & 31, h2 = lane >> 5;
#pragma unroll
            for (int ct = 0; ct < 2; ++ct)
#pragma unroll
                for (int tt = 0; tt < 2; ++tt)
#pragma unroll
                    for (int g4 = 0; g4 < 4; ++g4) {
                        const int tk = tt * 32 + t, chunk = ct * 8 + g4 * 2 + h2;
                        f32x4 v = {ac[ct][tt][4 * g4], ac[ct][tt][4 * g4 + 1], ac[ct][tt][4 * g4 + 2], ac[ct][tt][4 * g4 + 3]};
                        *(LAS f32x4*)(uintptr_t)(wave * 16384 + tk * 256 + ((chunk ^ (tk & 15)) * 16)) = v;
                    }
        }
        LGKM_BAR();
        TS(21);
        if (b == 0 && tid == 0) gstore8_dev(ws, WS_EPOCH, tagv);
        f32x4 s0 = {0.f, 0.f, 0.f, 0.f}, s1 = s0;
#pragma unroll
        for (int w = 0; w < 8; ++w) {
            s0 += *(const LAS f32x4*)(uintptr_t)(w * 16384 + tok * 256 + ((c0 ^ (tok & 15)) * 16));
            s1 += *(const LAS f32x4*)(uintptr_t)(w * 16384 + tok * 256 + (((c0 + 1) ^ (tok & 15)) * 16));
        }
        wait1<0>(xv);
        float xr[8];
#pragma unroll
        for (int i = 0; i < 4; ++i) { xr[2 * i] = __uint_as_float(xv[i] << 16); xr[2 * i + 1] = __uint_as_float(xv[i] & 0xffff0000u); }
        u32x4 o = {cvt_pk_bf16(s0[0] + xr[0], s0[1] + xr[1]), cvt_pk_bf16(s0[2] + xr[2], s0[3] + xr[3]),
                   cvt_pk_bf16(s1[0] + xr[4], s1[1] + xr[5]), cvt_pk_bf16(s1[2] + xr[6], s1[3] + xr[7])};
        *(u32x4*)((char*)out_ + off) = o;
    }
    TSOUT(0, 22);
}

extern "C" void kernel_launch(void* const* d_in, const int* in_sizes, int n_in, void* d_out, int out_size, void* d_ws, size_t ws_size,
                              hipStream_t stream) {
    const __hip_bfloat16* x = (const __hip_bfloat16*)d_in[0];
    const __hip_bfloat16* wqkv = (const __hip_bfloat16*)d_in[1];
    const __hip_bfloat16* wo = (const __hip_bfloat16*)d_in[2];
    const __hip_bfloat16* wg = (const __hip_bfloat16*)d_in[4];
    const __hip_bfloat16* wu = (const __hip_bfloat16*)d_in[5];
    const __hip_bfloat16* wd = (const __hip_bfloat16*)d_in[6];
    (void)hipFuncSetAttribute((const void*)k_fused, hipFuncAttributeMaxDynamicSharedMemorySize, LDS_BYTES);
    k_fused<<<256, 512, LDS_BYTES, stream>>>(x, wqkv, wo, wg, wu, wd, (char*)d_ws, (__hip_bfloat16*)d_out);
}
```

```cpp
#include <hip/hip_runtime.h>
#include <hip/hip_bf16.h>
#include <stdint.h>

typedef short bf16x8 __attribute__((ext_vector_type(8)));
typedef short s16x4 __attribute__((ext_vector_type(4)));
typedef float f32x2 __attribute__((ext_vector_type(2)));
typedef float f32x4 __attribute__((ext_vector_type(4)));
typedef float f32x16 __attribute__((ext_vector_type(16)));
typedef unsigned u32x4 __attribute__((ext_vector_type(4)));
typedef unsigned u32x2 __attribute__((ext_vector_type(2)));
typedef unsigned u32x16 __attribute__((ext_vector_type(16)));
typedef __bf16 bf16x2_t __attribute__((ext_vector_type(2)));
typedef unsigned long long u64;
#define LAS __attribute__((address_space(3)))
#define DI __device__ __forceinline__

constexpr int DM = 512;
constexpr int KSTEPS2 = 144;
constexpr unsigned WS_EPOCH = 0;
constexpr unsigned WS_FP = 262144;
constexpr unsigned WS_F2 = WS_FP + 262144;
constexpr unsigned WS_F3 = WS_F2 + 262144;
constexpr unsigned WS_ST = WS_F3 + 262144;
constexpr unsigned WS_CS = WS_ST + 262144;
constexpr unsigned WS_QKV = 2097152;
constexpr unsigned WS_A2  = WS_QKV + 6291456;
constexpr unsigned SPIN_CAP = 1u << 14;

DI unsigned cvt_pk_bf16(float lo, float hi) {
    f32x2 v = {lo, hi};
    bf16x2_t r = __builtin_convertvector(v, bf16x2_t);
    return __builtin_bit_cast(unsigned, r);
}
DI float dot2_bf16(unsigned a, unsigned b, float c) {
    return __builtin_amdgcn_fdot2_f32_bf16(__builtin_bit_cast(bf16x2_t, a), __builtin_bit_cast(bf16x2_t, b), c, false);
}
template <int IMM> DI void glds16i(const void* sbase, unsigned voff, unsigned ldsbase) {
    asm volatile("s_add_i32 m0, %2, %3\n\ts_nop 0\n\tglobal_load_lds_dwordx4 %0, %1"
                 :: "v"(voff), "s"(sbase), "s"(ldsbase), "n"(IMM) : "memory", "scc");
}
template <int IMM> DI void glds16i_dev(const void* sbase, unsigned voff, unsigned ldsbase) {
    asm volatile("s_add_i32 m0, %2, %3\n\ts_nop 0\n\tglobal_load_lds_dwordx4 %0, %1 sc1"
                 :: "v"(voff), "s"(sbase), "s"(ldsbase), "n"(IMM) : "memory", "scc");
}
template <int IMM> DI void glds16h_dev(const void* sbase, unsigned voff, unsigned ldsbase) {
    asm volatile("s_add_i32 m0, %2, %3\n\ts_mov_b32 exec_hi, 0\n\tglobal_load_lds_dwordx4 %0, %1 sc1\n\ts_mov_b32 exec_hi, -1"
                 :: "v"(voff), "s"(sbase), "s"(ldsbase), "n"(IMM) : "memory", "scc");
}
DI const char* spin(const char* p) { asm volatile("" : "+s"(p)); return p; }
template <int OFF, class T> DI void gload16(T& dst, const void* sbase, unsigned voff) {
    asm volatile("global_load_dwordx4 %0, %1, %2 offset:%3" : "=v"(dst) : "v"(voff), "s"(sbase), "n"(OFF) : "memory");
}
template <int OFF, class T> DI void gload16_dev(T& dst, const void* sbase, unsigned voff) {
    asm volatile("global_load_dwordx4 %0, %1, %2 offset:%3 sc1" : "=v"(dst) : "v"(voff), "s"(sbase), "n"(OFF) : "memory");
}
DI void gload8_dev(u32x2& dst, const void* sbase, unsigned voff) {
    asm volatile("global_load_dwordx2 %0, %1, %2 sc1" : "=v"(dst) : "v"(voff), "s"(sbase) : "memory");
}
template <int OFF = 0> DI void gstore16_dev(const void* sbase, unsigned voff, u32x4 v) {
    asm volatile("global_store_dwordx4 %0, %1, %2 offset:%3 sc1\n\ts_nop 1" :: "v"(voff), "v"(v), "s"(sbase), "n"(OFF) : "memory");
}
DI void gstore8_dev(const void* sbase, unsigned voff, u64 v) {
    asm volatile("global_store_dwordx2 %0, %1, %2 sc1" :: "v"(voff), "v"(v), "s"(sbase) : "memory");
}
DI void line_store(const void* sbase, unsigned lane16, u32x4 v) {
    asm volatile("s_mov_b64 exec, 0xff\n\tglobal_store_dwordx4 %0, %1, %2 sc1\n\ts_mov_b64 exec, -1\n\ts_nop 0" :: "v"(lane16), "v"(v), "s"(sbase) : "memory");
}
#define flag_store(ws, off, tag) line_store(spin(ws) + (off), lane16, u32x4{(unsigned)(tag), (unsigned)((tag) >> 32), (unsigned)(tag), (unsigned)((tag) >> 32)})
template <int N, class T> DI void wait4(T& a, T& b, T& c, T& d) {
    asm volatile("s_waitcnt vmcnt(%4)" : "+v"(a), "+v"(b), "+v"(c), "+v"(d) : "n"(N) : "memory");
}
template <int N, class T> DI void wait1(T& a) { asm volatile("s_waitcnt vmcnt(%1)" : "+v"(a) : "n"(N) : "memory"); }
template <int N> DI void waitv() { asm volatile("s_waitcnt vmcnt(%0)" :: "n"(N) : "memory"); }
DI bf16x8 lds_b128(unsigned addr) { return *(const LAS bf16x8*)(uintptr_t)addr; }
DI void lds_st128(unsigned addr, u32x4 v) { *(LAS u32x4*)(uintptr_t)addr = v; }
DI s16x4 lds_tr(unsigned addr) { return __builtin_amdgcn_ds_read_tr16_b64_v4i16((LAS s16x4*)(uintptr_t)addr); }
DI bf16x8 cat8(s16x4 a, s16x4 b) { return __builtin_shufflevector(a, b, 0, 1, 2, 3, 4, 5, 6, 7); }
template <int N> DI void waitv_bar() { asm volatile("s_waitcnt vmcnt(%0) lgkmcnt(0)\n\ts_barrier" :: "n"(N) : "memory"); }
template <int N> DI void waitv_bar0() { asm volatile("s_waitcnt vmcnt(%0)\n\ts_barrier" :: "n"(N) : "memory"); }
template <int N, int T> DI void waitT(int tsel) {
    asm volatile("s_cmp_lg_u32 %0, %2\n\ts_cbranch_scc1 1f\n\ts_waitcnt vmcnt(%1)\n1:" :: "s"(tsel), "n"(N), "n"(T) : "memory", "scc");
}
#define LGKM_BAR() asm volatile("s_waitcnt lgkmcnt(0)\n\ts_barrier" ::: "memory")
#define LGKM0() asm volatile("s_waitcnt lgkmcnt(0)" ::: "memory")
#define MFMA16(a, b, c) __builtin_amdgcn_mfma_f32_16x16x32_bf16(a, b, c, 0, 0, 0)
#define MFMA32(a, b, c) __builtin_amdgcn_mfma_f32_32x32x16_bf16(a, b, c, 0, 0, 0)
#define SBAR() __builtin_amdgcn_sched_barrier(0)
#define PIN(x) asm volatile("" : "+v"(x))
#define PIN2(x, y) asm volatile("" : "+v"(x), "+v"(y))
#define SPIN(x) asm volatile("" : "+s"(x))
#define SPIN2(x, y) asm volatile("" : "+s"(x), "+s"(y))
#define UNDEF(x) asm volatile("" : "=v"(x))


constexpr int AT_LDS_V = 0, AT_LDS_K = 98304;
constexpr int G2_SA = 131072, G2_SB = 65536, G2_SC = 32768;
constexpr int LDS_BYTES = 163840;

constexpr float QSCALE = 0.25f * 1.4426950408889634f;
constexpr float GC1 = -2.f * 0.7978845608028654f * 1.4426950408889634f, GC2 = GC1 * 0.044715f;

template <int C> DI float dpp_add(float v) { return v + __builtin_bit_cast(float, __builtin_amdgcn_update_dpp(0, __builtin_bit_cast(int, v), C, 0xf, 0xf, true)); }
DI float swap16_add(float v) { const auto s = __builtin_amdgcn_permlane16_swap(__float_as_uint(v), __float_as_uint(v), false, false); return __uint_as_float(s[0]) + __uint_as_float(s[1]); }
DI float swap32_add(float v) { const auto s = __builtin_amdgcn_permlane32_swap(__float_as_uint(v), __float_as_uint(v), false, false); return __uint_as_float(s[0]) + __uint_as_float(s[1]); }
DI unsigned umax3(unsigned a, unsigned b, unsigned c) { return max(max(a, b), c); }
DI unsigned umax_frag(unsigned m, bf16x8 f) { const u32x4 v = __builtin_bit_cast(u32x4, f); return umax3(umax3(m, v[0], v[1]), v[2], v[3]); }
DI float max3(float a, float b, float c) { return __builtin_fmaxf(__builtin_fmaxf(a, b), c); }
DI bool vis(int R) {
    const int bb = (R & 3) + 8 * (R >> 2);
    const u64 lo = 0xffffffffull & (0xffffffffull << bb), hi = 0xffffffffull & (0xffffffffull << (bb + 4));
    return __builtin_amdgcn_inverse_ballot_w64(lo | (hi << 32));
}
template <int SLEEP = 2> DI void poll_flags(const char* ws, unsigned foff, u32x2 v, u64 tag) {
    unsigned cnt = SPIN_CAP;
    asm volatile("1:\n\tv_cmp_ne_u64 vcc, %3, %0\n\ts_cbranch_vccz 2f\n\ts_sleep %5\n\tglobal_load_dwordx2 %0, %2, %4 sc1\n\ts_waitcnt vmcnt(0)\n\t"
                 "s_sub_u32 %1, %1, 1\n\ts_cbranch_scc0 1b\n2:" : "+v"(v), "+s"(cnt) : "v"(foff), "s"(tag), "s"(ws), "n"(SLEEP) : "memory", "vcc", "scc");
}

__global__ __launch_bounds__(512, 2) void k_fused(const void* a0_, const void* a1_, const void* a2_, const void* a3_, const void* a4_,
        const void* a5_, void* a6_, void* a7_) {
    u32x16 ka;
    asm volatile("s_load_dwordx16 %0, %1, 0x0" : "=s"(ka) : "s"(__builtin_amdgcn_kernarg_segment_ptr()) : "memory");
    SBAR();
    const int b = blockIdx.x, xcd = b & 7, idx = b >> 3;
    const int tid = threadIdx.x, lane = tid & 63;
    const int wave = __builtin_amdgcn_readfirstlane(tid >> 6);
    const int tm2 = xcd * 4 + (idx >> 3), tn2 = idx & 7;
    const int head = xcd * 4 + (idx & 3), tg = idx >> 2, qt = 8 * tg + wave;
    const int tm = (xcd >> 2) * 8 + (idx >> 2), cq = xcd & 3, cp = idx & 3, tn = cq * 4 + cp;
    const int tq = wave & 3, ch = wave >> 2;
    int tsel = (cp + 1) >> 2;
    const int qu0 = 6 * cq + cp, f1 = 7 * cq + 2 * cp, f2 = f1 + 1 - tsel;
    const int t32 = tm * 4 + tq, kp = tm & 3, rot = 2 * kp;
    unsigned wave1k = wave * 1024, wave2k = wave * 2048, wave4k = wave * 4096;
    unsigned lane16 = lane * 16;
    unsigned tsink = 0;
    const unsigned strM = 3584u - 512u * tsel, stepM = 64 * strM, nstepM = 0u - 7 * stepM;
    const unsigned colw = (unsigned)(((lane & 7) ^ (((lane >> 4) & 1) << 2)) * 16);
    unsigned vx0 = (tm * 128 + 16 * wave + (lane >> 3)) * 1024 + rot * 128 + (((lane & 7) ^ (lane >> 4)) * 16), vx1 = (vx0 ^ 64) + 8192;
    const unsigned wrow = rot * 64 + 8 * wave + (lane >> 3);
    unsigned v0 = wrow * 3072u + colw + qu0 * 128, vM = wrow * strM + colw + (tsel ? qu0 + 1 : f1) * 128, vF = wrow * 3584u + colw + f2 * 128;
    PIN2(vx0, vx1); PIN2(v0, vM); PIN2(vF, lane16); SPIN2(wave1k, wave2k); SPIN(tsel); SBAR();
    asm volatile("s_waitcnt lgkmcnt(0)" : "+s"(ka) :: "memory");
#define KARG(i) ((u64)ka[2 * (i) + 1] << 32 | ka[2 * (i)])
    const char* xp = (const char*)KARG(0);
    const char* sb0 = (const char*)KARG(1);
    const char* sb3 = (const char*)KARG(3);
    const char* sb4 = (const char*)KARG(4);
    const char* wo_ = (const char*)KARG(2);
    const char* wd_ = (const char*)KARG(5);
    char* ws = (char*)KARG(6);
    const char* out_ = (const char*)KARG(7);
    const char* sb1 = (const char*)(tsel ? (u64)sb0 : (u64)sb3);
    const char* sb2 = (const char*)(tsel ? (u64)sb0 + 128 : (u64)sb4);
    u32x4 xr;
    u64 tagv = *(const __attribute__((address_space(4))) u64*)(uintptr_t)(ws + WS_EPOCH);
    unsigned step;
#define SIDELOADS { SBAR(); step = ((unsigned)tagv + 1) & 7; SPIN2(ws, step); \
        gload16<0>(xr, xp + (tm * 128 + tn * 8 + wave) * 1024, lane16); \
        const unsigned cb = WS_CS + ((tm >> 2) * 16 + tn) * 10240 + (kp & 2) * 256; \
        unsigned ua = cb + (6 + 2 * ch + (tq & 1)) * 1024, ub = cb + (3 * ch + (tq == 3 ? 0 : tq)) * 1024; SPIN2(ua, ub); \
        unsigned a0 = ((lane & 4) ? ua : ub) + (lane & 3) * 128; PIN(a0); \
        asm volatile("global_load_dword %0, %1, %2 sc1" : "+v"(tsink) : "v"(lane >= (tq < 2 ? 8 : tq == 2 ? 4 : 0) ? WS_ST + (tm * 128 + tn * 8) * 16 : a0), "s"(ws) : "memory"); }
#define TOUCHALL { PIN(lane16); const u64 t0_ = (u64)wd_ + (b * 8 + wave) * 896, t1_ = (u64)wo_ + (b * 8 + wave) * 256; \
        const u64 ta_ = (lane16 < 512 ? t0_ : t1_) + min(lane16 * 8, lane16 < 512 ? 768u : (lane16 & 16) * 8); \
        asm volatile("global_load_dword %0, %1, off" : "+v"(tsink) : "v"(ta_) : "memory"); }
    unsigned tag30; u32x4 stat; float s1, s2;
#define STATSTORE asm volatile("s_mov_b64 exec, 1\n\tglobal_store_dwordx4 %0, %1, %2 sc1\n\ts_mov_b64 exec, -1\n\ts_nop 0" :: "v"(lane16), "v"(stat), "s"(ws + WS_ST + (tm * 128 + tn * 8 + wave) * 16) : "memory")

    const f32x16 zero16 = {};
    const f32x4 zero4 = {};
    f32x16 acc[3], acc3, acc4;
    float sg[16]; u32x2 pq; unsigned vU;
    bf16x8 xf[32], ca0, ca1, ca2, stv, kv0, kv1, kv2, kv3, qf;
    s16x4 wf0a, wf0b, wf1a, wf1b, wf2a, wf2b, wf3a, wf3b, wf4a, wf4b, wf5a, wf5b, cf0a, cf0b, cf1a, cf1b, cf2a, cf2b;
    f32x4 cs;
    float t0, t1, p0, p1, qs, gk1, gk2, gvar;
    unsigned hp0, hp1, hp2, hp3, csw, mh, ml, bad;
    const u32x4 poison = {~0u, ~0u, ~0u, ~0u};
#define CSPACK { const float lo2 = (lane16 & 32) ? cs[1] : cs[0], hi2 = (lane16 & 32) ? cs[3] : cs[2], v = (lane16 & 64) ? hi2 : lo2; \
        csw = cvt_pk_bf16(v, v - __uint_as_float(cvt_pk_bf16(v, v) & 0xffff0000u)); PIN(csw); }
#define CSSTORE(dst, on) asm volatile("s_mov_b64 exec, %3\n\tglobal_store_dwordx2 %0, %1, %2 sc1\n\ts_mov_b64 exec, -1" :: "v"(cw_off), "v"((u64)tag30 << 32 | csw), "s"(dst), "s"((on) ? 0x00ff00ff00ff00ffull : 1ull) : "memory")
#define T1LOAD glds16i_dev<8192>(t1src, lane16, wave1k)
#define T2LOAD glds16h_dev<0>(spin(cstab) + (6144 + (wave1k >> 1)), lane16, wave1k >> 1)
#define T1READ { PIN2(lane16, st_off); const unsigned ta_ = lane16 + ch * 3072; ca0 = lds_b128(ta_ + 8192); ca1 = lds_b128(ta_ + 8192 + 1024); ca2 = lds_b128(ta_ + 8192 + 2048); stv = lds_b128((st_off & 2047u) + 8192 + 6144); }
#define T2READ { PIN(lane16); const unsigned ta_ = lane16 + ch * 2048; ca0 = lds_b128(ta_ + 0); ca1 = lds_b128(ta_ + 0 + 1024); PIN2(mh, ml); }
#define BADTAG(f) ((__builtin_bit_cast(u32x4, f)[1] ^ tag30) | (__builtin_bit_cast(u32x4, f)[3] ^ tag30))
#define BAD1 { bad = BADTAG(ca0) | BADTAG(ca1) | BADTAG(ca2) | BADTAG(stv); PIN(bad); }
#define BAD2 { bad = BADTAG(ca0) | BADTAG(ca1); PIN(bad); }
#define REFETCH(V, VO, SB, OFF) "s_mov_b32 %[cnt], %[cap]\n1:\n\ts_sleep 2\n\tglobal_load_dword %[t0], %[" VO "], %[" SB "] offset:" OFF "+4 sc1\n\tglobal_load_dword %[t1], %[" VO "], %[" SB "] offset:" OFF "+12 sc1\n\t" \
        "s_waitcnt vmcnt(0)\n\tv_xor_b32 %[t0], %[tag], %[t0]\n\tv_xor_b32 %[t1], %[tag], %[t1]\n\tv_or_b32 %[t0], %[t0], %[t1]\n\tv_cmp_ne_u32 vcc, 0, %[t0]\n\ts_cbranch_vccz 3f\n\t" \
        "s_sub_u32 %[cnt], %[cnt], 1\n\ts_cbranch_scc0 1b\n3:\n\tglobal_load_dwordx4 %[" V "], %[" VO "], %[" SB "] offset:" OFF " sc1\n\t"
#define CHECK1 { unsigned q0_, q1_, cnt_; asm volatile("v_cmp_ne_u32 vcc, 0, %[bad]\n\ts_cbranch_vccz 2f\n\t" REFETCH("st", "so", "ws", "0") REFETCH("a0", "lo", "cs", "0") REFETCH("a1", "lo", "cs", "1024") REFETCH("a2", "lo", "cs", "2048") \
        "s_waitcnt vmcnt(0)\n2:" : [st] "+v"(stv), [a0] "+v"(ca0), [a1] "+v"(ca1), [a2] "+v"(ca2), [t0] "=&v"(q0_), [t1] "=&v"(q1_), [cnt] "=&s"(cnt_) \
        : [bad] "v"(bad), [so] "v"(st_off), [lo] "v"(lane16), [ws] "s"(ws), [cs] "s"(spin(cstab) + ch * 3072), [tag] "s"(tag30), [cap] "n"(SPIN_CAP) : "memory", "vcc", "scc"); }
#define CHECK2 { unsigned q0_, q1_, cnt_; asm volatile("v_cmp_ne_u32 vcc, 0, %[bad]\n\ts_cbranch_vccz 2f\n\t" REFETCH("a0", "lo", "cs", "0") REFETCH("a1", "lo", "cs", "1024") \
        "s_waitcnt vmcnt(0)\n2:" : [a0] "+v"(ca0), [a1] "+v"(ca1), [t0] "=&v"(q0_), [t1] "=&v"(q1_), [cnt] "=&s"(cnt_) \
        : [bad] "v"(bad), [lo] "v"(lane16), [cs] "s"(spin(cstab) + (6144 + ch * 2048)), [tag] "s"(tag30), [cap] "n"(SPIN_CAP) : "memory", "vcc", "scc"); }
#define STATS { const float nm = -__uint_as_float(__builtin_bit_cast(u32x4, stv)[0]); gvar = __uint_as_float(__builtin_bit_cast(u32x4, stv)[2]); \
        const float rs = __builtin_amdgcn_rsqf(gvar); \
        qs = __uint_as_float(sc0) * rs; gk1 = GC1 * rs; gk2 = GC2 * rs * rs * rs; \
        mh = cvt_pk_bf16(nm, nm); const float lo = nm - __uint_as_float(mh & 0xffff0000u); \
        ml = cvt_pk_bf16(lo, lo); PIN2(qs, gk1); PIN2(gk2, gvar); PIN2(mh, ml); }
#define MEAN __builtin_bit_cast(bf16x8, u32x4{mh, ml, mh, ml})
#define CDUP(f) __builtin_shufflevector(f, f, 0, 1, 0, 1, 4, 5, 4, 5)
#define UNIT(P) ((P) < 3 ? (ue >> (4 * (P))) & 7u : (ul >> (4 * (((P) - 3) & 3))) & 7u)
#define KVLOAD0 gload16_dev<0>(kv0, spin(Qp) + (2097152 - far), vk0)
#define KVLOAD1 gload16_dev<0>(kv1, spin(Qp) + 2097152, lane16)
#define KVLOAD2 gload16_dev<0>(kv2, spin(Qp) + (4194304 - far), vv)
#define KVLOAD3 gload16_dev<0>(kv3, spin(Qp) + 4194304, vv)
#define KVLOAD4 gload16_dev<0>(qf, Qp, lane16)
#define op (g2r & 1)
#define slot_of(P) ((P) % 3 == 0 ? G2_SA : (P) % 3 == 1 ? G2_SB : G2_SC)
    const char *wb, *ab;
#define WBASE(P) wb = (P) < 7 ? spin(wd) + UNIT(P) * 65536 : spin(wo) + (((P) - 7) ^ op) * 32768; SPIN(wb)
#define ABASE(P) ab = (P) < 7 ? spin(a2h) + UNIT(P) * 4096 : spin(a2o) + (((P) - 7) ^ op) * 2048; SPIN(ab)
#define SETM0(P) asm volatile("s_add_i32 m0, %0, %1" :: "s"(wave4k), "n"(slot_of(P)) : "memory", "scc")
#define issueW(k) asm volatile("global_load_lds_dwordx4 %0, %1 offset:%2" :: "v"(vs##k), "s"(wb), "n"((k) * 1024) : "memory")
#define issueA(P, k) gload16_dev<((k) >> 1) * 1024>(ta[P][k], ab, (k) & 1 ? vb2 : lane16)
#define vs0 vsrc2
    s16x4 w2h[9][4][2];
#define rdw(P, k) w2h[P][(k) >> 1][(k) & 1] = lds_tr((slot_of(P) == G2_SA ? ((k) & 2 ? rpA1 : rpA0) : ((k) & 2 ? rpC1 : rpC0) + (slot_of(P) - G2_SC)) + ((P) >= 7 ? hh512 : 0u) + ((k) >> 2) * 2048 + ((k) & 1) * ((P) < 7 ? 1024 : 512))
    glds16i<0>(xp, vx0, wave2k); vx0 += 128u; PIN(vx0);
    glds16i<1024>(xp, vx1, wave2k); vx1 += 128u; PIN(vx1);
    glds16i<65536>(sb0, v0, wave1k); v0 += 196608u; PIN(v0);
    glds16i<73728>(sb1, vM, wave1k);
    glds16i<81920>(sb2, vM, wave1k); vM += stepM; PIN(vM);
    SBAR();
    unsigned xa0, xa1, xa2, xa3, wa0, wa1, wa2, wc;
    bf16x8 cpat;
    {
        const int hh = lane >> 5, row = 32 * tq + (lane & 31);
        xa0 = (unsigned)(row * 128 + ((hh ^ ((row >> 1) & 7)) * 16)); xa1 = xa0 ^ 32; xa2 = xa0 ^ 64; xa3 = xa0 ^ 96;
        const int nb = (lane >> 4) & 1, r = (lane & 15) >> 2, p = lane & 3;
        const unsigned rdb = (unsigned)(r * 128 + (((r >> 1) & 1) << 6) + nb * 32 + p * 8);
        wa0 = (rdb ^ (ch << 6)) + hh * 1024; wa1 = wa0 + 65536; wa2 = wa0 + 131072;
        wc = wa1 + (tq == 3 ? 0 : tq) * 8192;
        const unsigned cv = (((lane >> 4) ^ lane) & 1) ? 0u : 0x3f803f80u;
        cpat = __builtin_bit_cast(bf16x8, u32x4{cv, cv, cv, cv});
    }
    PIN(xa0); PIN(xa1); PIN(xa2); PIN(xa3); PIN(wa0); PIN(wa1); PIN(wa2); PIN(wc); PIN(cpat);
    SBAR();
    asm volatile("s_barrier" ::: "memory");
        glds16i<16384>(xp, vx0, wave2k); vx0 += rot == 6 ? 0u - 896u : 128u; PIN(vx0);
    glds16i<17408>(xp, vx1, wave2k); vx1 += rot == 6 ? 0u - 896u : 128u; PIN(vx1);
    glds16i<90112>(sb0, v0, wave1k); v0 += rot == 6 ? 0u - 7 * 196608u : 196608u; PIN(v0);
    glds16i<98304>(sb1, vM, wave1k);
    glds16i<106496>(sb2, vM, wave1k); vM += rot == 6 ? nstepM : stepM; PIN(vM);
    SIDELOADS;
    tagv += 0x9E3779B97F4A7C15ull; tagv = (tagv ^ (tagv >> 30)) * 0xBF58476D1CE4E5B9ull; SPIN(tagv);
    tagv = (tagv ^ (tagv >> 27)) * 0x94D049BB133111EBull; SPIN(tagv);
    tagv ^= tagv >> 31; tagv = (tagv & ~7ull) | step; tag30 = (unsigned)tagv & 0xbfffbfffu; SPIN2(tagv, tag30);
    const char* cstab = ws + (WS_CS + (unsigned)(((tm >> 2) * 16 + tn) * 10240)); SPIN(cstab);
    const char* cs1dst = cstab + (unsigned)((3 * ch + (tq == 3 ? 0 : tq)) * 1024 + (kp & 2) * 256 + (kp & 1) * 8); SPIN(cs1dst);
    const char* t1src = (wave < 6 ? cstab : ws + (WS_ST - 6144 + (unsigned)tm * 2048)) + wave1k; SPIN(t1src);
    unsigned cw_off = (unsigned)((16 * (lane & 1) + 4 * (lane >> 4) + ((lane >> 1) & 3)) * 16); PIN(cw_off);
    const char* odst = ws + (WS_A2 + (unsigned)(qt * KSTEPS2 + head) * 1024); SPIN(odst);
    unsigned st_off = WS_ST + (t32 * 32 + (lane & 31)) * 16; PIN(st_off);
    unsigned sc0 = qu0 < 8 ? __builtin_bit_cast(unsigned, QSCALE) : 0x3f800000u; SPIN2(sc0, wave4k);
    const char* cs2dst = cstab + (unsigned)((6 + 2 * ch + (tq & 1)) * 1024 + (kp & 2) * 256 + (kp & 1) * 8); SPIN(cs2dst);
    unsigned wc2a = wa1 + (tq & 1) * 8192, wc2b = wa0 + ((tq & 1) ? 0 : 81920); PIN2(wc2a, wc2b);
    const char* qdst = ws + (WS_QKV + (unsigned)((qu0 * 4 + 2 * ch) * 64 + t32) * 1024); SPIN(qdst);
    const char* hdst = ws + (WS_A2 + (unsigned)(t32 * KSTEPS2 + 32 + 4 * f1 + 2 * ch) * 1024); SPIN(hdst);
    const char* wd = wd_ + (unsigned)(((wave >> 1) * 448 + (wave & 1) * 32) * 1024 + tn2 * 128); SPIN(wd);
    unsigned g2r = tn2 + tm2, ue = 0x20420u >> (4 * (g2r % 3)), ul = 0x5316531u >> (4 * (g2r & 3)); SPIN2(ue, ul);
    unsigned myflag = (unsigned)(((tm * 16 + tn) * 8 + wave) * 128), far = tg > 0 ? 8192u : wave1k; SPIN2(far, myflag);
    const char* Qp = ws + (WS_QKV + (unsigned)(head * 64 + qt) * 1024); SPIN(Qp);
        waitv_bar0<7>();
        wf0a = lds_tr(wa1 + 0);
    wf0b = lds_tr(wa1 + 512);
    xf[0] = lds_b128(xa0 + 0);
    wf1a = lds_tr(wa1 + 2048);
    wf1b = lds_tr(wa1 + 2560);
    xf[1] = lds_b128(xa1 + 0);
    wf2a = lds_tr(wa1 + 4096);
    wf2b = lds_tr(wa1 + 4608);
    xf[2] = lds_b128(xa2 + 0);
    wf3a = lds_tr(wa1 + 6144);
    wf3b = lds_tr(wa1 + 6656);
    xf[3] = lds_b128(xa3 + 0);
    wf4a = lds_tr(wa1 + 8192);
    wf4b = lds_tr(wa1 + 8704);
    SBAR();
    acc[0] = MFMA32(cat8(wf0a, wf0b), xf[0], zero16); PIN(acc[0]); SBAR();
    wf5a = lds_tr(wa1 + 10240);
    wf5b = lds_tr(wa1 + 10752);
    cf0a = lds_tr(wc + 0);
    cf0b = lds_tr(wc + 0 + 512);
    glds16i<32768>(xp, vx0, wave2k); vx0 += 128u; PIN(vx0);
    SBAR();
    acc[0] = MFMA32(cat8(wf1a, wf1b), xf[1], acc[0]); PIN(acc[0]); SBAR();
    wf0a = lds_tr(wa1 + 12288);
    wf0b = lds_tr(wa1 + 12800);
    cf1a = lds_tr(wc + 2048);
    cf1b = lds_tr(wc + 2048 + 512);
    glds16i<33792>(xp, vx1, wave2k); vx1 += 128u; PIN(vx1);
    SBAR();
    acc[0] = MFMA32(cat8(wf2a, wf2b), xf[2], acc[0]); PIN(acc[0]); SBAR();
    wf1a = lds_tr(wa1 + 14336);
    wf1b = lds_tr(wa1 + 14848);
    cf2a = lds_tr(wc + 4096);
    cf2b = lds_tr(wc + 4096 + 512);
    glds16i<114688>(sb0, v0, wave1k); v0 += 196608u; PIN(v0);
    SBAR();
    cs = MFMA16(cat8(cf0a, cf0b), cpat, zero4); PIN(cs); SBAR();
    acc[0] = MFMA32(cat8(wf3a, wf3b), xf[3], acc[0]); PIN(acc[0]); SBAR();
    wf2a = lds_tr(wa1 + 16384);
    wf2b = lds_tr(wa1 + 16896);
    cf0a = lds_tr(wc + 6144);
    cf0b = lds_tr(wc + 6144 + 512);
    glds16i<122880>(sb1, vM, wave1k);
    SBAR();
    cs = MFMA16(cat8(cf1a, cf1b), cpat, cs); PIN(cs); SBAR();
    acc[1] = MFMA32(cat8(wf4a, wf4b), xf[0], zero16); PIN(acc[1]); SBAR();
    wf3a = lds_tr(wa1 + 18432);
    wf3b = lds_tr(wa1 + 18944);
    glds16i<131072>(sb2, vM, wave1k); vM += stepM; PIN(vM);
    SBAR();
    cs = MFMA16(cat8(cf2a, cf2b), cpat, cs); PIN(cs); SBAR();
    acc[1] = MFMA32(cat8(wf5a, wf5b), xf[1], acc[1]); PIN(acc[1]); SBAR();
    wf4a = lds_tr(wa1 + 20480);
    wf4b = lds_tr(wa1 + 20992);
    glds16i<49152>(xp, vx0, wave2k); vx0 += rot == 4 ? 0u - 896u : 128u; PIN(vx0);
    SBAR();
    cs = MFMA16(cat8(cf0a, cf0b), cpat, cs); PIN(cs); SBAR();
    acc[1] = MFMA32(cat8(wf0a, wf0b), xf[2], acc[1]); PIN(acc[1]); SBAR();
    wf5a = lds_tr(wa1 + 22528);
    wf5b = lds_tr(wa1 + 23040);
    glds16i<50176>(xp, vx1, wave2k); vx1 += rot == 4 ? 0u - 896u : 128u; PIN(vx1);
    SBAR();
    acc[1] = MFMA32(cat8(wf1a, wf1b), xf[3], acc[1]); PIN(acc[1]); SBAR();
    glds16i<139264>(sb0, v0, wave1k); v0 += rot == 4 ? 0u - 7 * 196608u : 196608u; PIN(v0);
    SBAR();
    waitv_bar<10>();
        wait1<8>(xr);
    acc[2] = MFMA32(cat8(wf2a, wf2b), xf[0], zero16); PIN(acc[2]); SBAR();
    wf0a = lds_tr(wa1 + 24576);
    wf0b = lds_tr(wa1 + 25088);
    xf[4] = lds_b128(xa0 + 16384);
    s1 = dot2_bf16(xr[0], 0x3f803f80u, 0.f); s2 = dot2_bf16(xr[0], xr[0], 0.f); PIN2(s1, s2);
    glds16i<147456>(sb1, vM, wave1k);
    glds16i<155648>(sb2, vM, wave1k); vM += rot == 4 ? nstepM : stepM; PIN(vM);
    SBAR();
    acc[2] = MFMA32(cat8(wf3a, wf3b), xf[1], acc[2]); PIN(acc[2]); SBAR();
    wf1a = lds_tr(wa1 + 26624);
    wf1b = lds_tr(wa1 + 27136);
    xf[5] = lds_b128(xa1 + 16384);
    s1 = dot2_bf16(xr[1], 0x3f803f80u, s1); s2 = dot2_bf16(xr[1], xr[1], s2); PIN2(s1, s2);
    glds16i<0>(xp, vx0, wave2k); vx0 += 128u; PIN(vx0);
    SBAR();
    acc[2] = MFMA32(cat8(wf4a, wf4b), xf[2], acc[2]); PIN(acc[2]); SBAR();
    wf2a = lds_tr(wa1 + 28672);
    wf2b = lds_tr(wa1 + 29184);
    xf[6] = lds_b128(xa2 + 16384);
    s1 = dot2_bf16(xr[2], 0x3f803f80u, s1); s2 = dot2_bf16(xr[2], xr[2], s2); PIN2(s1, s2);
    SBAR();
    acc[2] = MFMA32(cat8(wf5a, wf5b), xf[3], acc[2]); PIN(acc[2]); SBAR();
    wf3a = lds_tr(wa1 + 30720);
    wf3b = lds_tr(wa1 + 31232);
    xf[7] = lds_b128(xa3 + 16384);
    s1 = dot2_bf16(xr[3], 0x3f803f80u, s1); s2 = dot2_bf16(xr[3], xr[3], s2); PIN2(s1, s2);
    glds16i<1024>(xp, vx1, wave2k); vx1 += 128u; PIN(vx1);
    SBAR();
    acc[0] = MFMA32(cat8(wf0a, wf0b), xf[4], acc[0]); PIN(acc[0]); SBAR();
    wf4a = lds_tr(wa1 + 32768);
    wf4b = lds_tr(wa1 + 33280);
    wf5a = lds_tr(wa1 + 34816);
    wf5b = lds_tr(wa1 + 35328);
    s1 = dpp_add<0xB1>(s1); PIN2(s1, s2); s2 = dpp_add<0xB1>(s2); PIN2(s1, s2); s1 = dpp_add<0x4E>(s1); PIN2(s1, s2); s2 = dpp_add<0x4E>(s2); PIN2(s1, s2);
    SBAR();
    acc[0] = MFMA32(cat8(wf1a, wf1b), xf[5], acc[0]); PIN(acc[0]); SBAR();
    wf0a = lds_tr(wa1 + 36864);
    wf0b = lds_tr(wa1 + 37376);
    cf1a = lds_tr(wc + 24576);
    cf1b = lds_tr(wc + 24576 + 512);
    s1 = dpp_add<0x141>(s1); PIN2(s1, s2); s2 = dpp_add<0x141>(s2); PIN2(s1, s2); s1 = dpp_add<0x140>(s1); PIN2(s1, s2); s2 = dpp_add<0x140>(s2); PIN2(s1, s2);
    glds16i<65536>(sb0, v0, wave1k); v0 += 196608u; PIN(v0);
    SBAR();
    acc[0] = MFMA32(cat8(wf2a, wf2b), xf[6], acc[0]); PIN(acc[0]); SBAR();
    wf1a = lds_tr(wa1 + 38912);
    wf1b = lds_tr(wa1 + 39424);
    cf2a = lds_tr(wc + 26624);
    cf2b = lds_tr(wc + 26624 + 512);
    s1 = swap16_add(s1); PIN2(s1, s2); s2 = swap16_add(s2); PIN2(s1, s2);
    SBAR();
    acc[0] = MFMA32(cat8(wf3a, wf3b), xf[7], acc[0]); PIN(acc[0]); SBAR();
    wf2a = lds_tr(wa1 + 40960);
    wf2b = lds_tr(wa1 + 41472);
    cf0a = lds_tr(wc + 28672);
    cf0b = lds_tr(wc + 28672 + 512);
    s1 = swap32_add(s1); PIN2(s1, s2); s2 = swap32_add(s2); PIN2(s1, s2);
    glds16i<73728>(sb1, vM, wave1k);
    SBAR();
    cs = MFMA16(cat8(cf1a, cf1b), cpat, cs); PIN(cs); SBAR();
    acc[1] = MFMA32(cat8(wf4a, wf4b), xf[4], acc[1]); PIN(acc[1]); SBAR();
    wf3a = lds_tr(wa1 + 43008);
    wf3b = lds_tr(wa1 + 43520);
    cf1a = lds_tr(wc + 30720);
    cf1b = lds_tr(wc + 30720 + 512);
    s1 *= 1.f / 512.f; PIN2(s1, s2); s2 = fmaxf(s2 * (1.f / 512.f) - s1 * s1, 0.f) + 1e-5f; stat = u32x4{__float_as_uint(s1), tag30, __float_as_uint(s2), tag30}; PIN(stat);
    SBAR();
    cs = MFMA16(cat8(cf2a, cf2b), cpat, cs); PIN(cs); SBAR();
    acc[1] = MFMA32(cat8(wf5a, wf5b), xf[5], acc[1]); PIN(acc[1]); SBAR();
    wf4a = lds_tr(wa1 + 45056);
    wf4b = lds_tr(wa1 + 45568);
    glds16i<81920>(sb2, vM, wave1k); vM += stepM; PIN(vM);
    STATSTORE;
    SBAR();
    cs = MFMA16(cat8(cf0a, cf0b), cpat, cs); PIN(cs); SBAR();
    acc[1] = MFMA32(cat8(wf0a, wf0b), xf[6], acc[1]); PIN(acc[1]); SBAR();
    wf5a = lds_tr(wa1 + 47104);
    wf5b = lds_tr(wa1 + 47616);
    SBAR();
    cs = MFMA16(cat8(cf1a, cf1b), cpat, cs); PIN(cs); SBAR();
    acc[1] = MFMA32(cat8(wf1a, wf1b), xf[7], acc[1]); PIN(acc[1]); SBAR();
    __builtin_amdgcn_s_sleep(12);
    SBAR();
    waitv_bar<11>();
        acc[2] = MFMA32(cat8(wf2a, wf2b), xf[4], acc[2]); PIN(acc[2]); SBAR();
    wf0a = lds_tr(wa1 + 49152);
    wf0b = lds_tr(wa1 + 49664);
    xf[8] = lds_b128(xa0 + 32768);
    gstore16_dev(odst, lane16, poison);
    SBAR();
    acc[2] = MFMA32(cat8(wf3a, wf3b), xf[5], acc[2]); PIN(acc[2]); SBAR();
    wf1a = lds_tr(wa1 + 51200);
    wf1b = lds_tr(wa1 + 51712);
    xf[9] = lds_b128(xa1 + 32768);
    CSPACK;
    glds16i<16384>(xp, vx0, wave2k); vx0 += rot == 2 ? 0u - 896u : 128u; PIN(vx0);
    SBAR();
    acc[2] = MFMA32(cat8(wf4a, wf4b), xf[6], acc[2]); PIN(acc[2]); SBAR();
    wf2a = lds_tr(wa1 + 53248);
    wf2b = lds_tr(wa1 + 53760);
    xf[10] = lds_b128(xa2 + 32768);
    CSSTORE(cs1dst, tq < 3);
    SBAR();
    acc[2] = MFMA32(cat8(wf5a, wf5b), xf[7], acc[2]); PIN(acc[2]); SBAR();
    wf3a = lds_tr(wa1 + 55296);
    wf3b = lds_tr(wa1 + 55808);
    xf[11] = lds_b128(xa3 + 32768);
    glds16i<17408>(xp, vx1, wave2k); vx1 += rot == 2 ? 0u - 896u : 128u; PIN(vx1);
    SBAR();
    acc[0] = MFMA32(cat8(wf0a, wf0b), xf[8], acc[0]); PIN(acc[0]); SBAR();
    wf4a = lds_tr(wa1 + 57344);
    wf4b = lds_tr(wa1 + 57856);
    wf5a = lds_tr(wa1 + 59392);
    wf5b = lds_tr(wa1 + 59904);
    SBAR();
    acc[0] = MFMA32(cat8(wf1a, wf1b), xf[9], acc[0]); PIN(acc[0]); SBAR();
    wf0a = lds_tr(wa1 + 61440);
    wf0b = lds_tr(wa1 + 61952);
    glds16i<90112>(sb0, v0, wave1k); v0 += rot == 2 ? 0u - 7 * 196608u : 196608u; PIN(v0);
    SBAR();
    acc[0] = MFMA32(cat8(wf2a, wf2b), xf[10], acc[0]); PIN(acc[0]); SBAR();
    wf1a = lds_tr(wa1 + 63488);
    wf1b = lds_tr(wa1 + 64000);
    SBAR();
    acc[0] = MFMA32(cat8(wf3a, wf3b), xf[11], acc[0]); PIN(acc[0]); SBAR();
    wf2a = lds_tr(wa2 + 0);
    wf2b = lds_tr(wa2 + 512);
    glds16i<98304>(sb1, vM, wave1k);
    SBAR();
    acc[1] = MFMA32(cat8(wf4a, wf4b), xf[8], acc[1]); PIN(acc[1]); SBAR();
    wf3a = lds_tr(wa2 + 2048);
    wf3b = lds_tr(wa2 + 2560);
    SBAR();
    acc[1] = MFMA32(cat8(wf5a, wf5b), xf[9], acc[1]); PIN(acc[1]); SBAR();
    wf4a = lds_tr(wa2 + 4096);
    wf4b = lds_tr(wa2 + 4608);
    glds16i<106496>(sb2, vM, wave1k); vM += rot == 2 ? nstepM : stepM; PIN(vM);
    SBAR();
    acc[1] = MFMA32(cat8(wf0a, wf0b), xf[10], acc[1]); PIN(acc[1]); SBAR();
    wf5a = lds_tr(wa2 + 6144);
    wf5b = lds_tr(wa2 + 6656);
    SBAR();
    acc[1] = MFMA32(cat8(wf1a, wf1b), xf[11], acc[1]); PIN(acc[1]); SBAR();
    SBAR();
    waitv_bar<13>();
        acc[2] = MFMA32(cat8(wf2a, wf2b), xf[8], acc[2]); PIN(acc[2]); SBAR();
    wf0a = lds_tr(wa2 + 8192);
    wf0b = lds_tr(wa2 + 8704);
    xf[12] = lds_b128(xa0 + 49152);
    SBAR();
    acc[2] = MFMA32(cat8(wf3a, wf3b), xf[9], acc[2]); PIN(acc[2]); SBAR();
    wf1a = lds_tr(wa2 + 10240);
    wf1b = lds_tr(wa2 + 10752);
    xf[13] = lds_b128(xa1 + 49152);
    glds16i<32768>(xp, vx0, wave2k); vx0 += 128u; PIN(vx0);
    SBAR();
    acc[2] = MFMA32(cat8(wf4a, wf4b), xf[10], acc[2]); PIN(acc[2]); SBAR();
    wf2a = lds_tr(wa2 + 12288);
    wf2b = lds_tr(wa2 + 12800);
    xf[14] = lds_b128(xa2 + 49152);
    SBAR();
    acc[2] = MFMA32(cat8(wf5a, wf5b), xf[11], acc[2]); PIN(acc[2]); SBAR();
    wf3a = lds_tr(wa2 + 14336);
    wf3b = lds_tr(wa2 + 14848);
    xf[15] = lds_b128(xa3 + 49152);
    glds16i<33792>(xp, vx1, wave2k); vx1 += 128u; PIN(vx1);
    SBAR();
    acc[0] = MFMA32(cat8(wf0a, wf0b), xf[12], acc[0]); PIN(acc[0]); SBAR();
    wf4a = lds_tr(wa2 + 16384);
    wf4b = lds_tr(wa2 + 16896);
    wf5a = lds_tr(wa2 + 18432);
    wf5b = lds_tr(wa2 + 18944);
    SBAR();
    acc[0] = MFMA32(cat8(wf1a, wf1b), xf[13], acc[0]); PIN(acc[0]); SBAR();
    wf0a = lds_tr(wa2 + 20480);
    wf0b = lds_tr(wa2 + 20992);
    glds16i<114688>(sb0, v0, wave1k); v0 += 196608u; PIN(v0);
    SBAR();
    acc[0] = MFMA32(cat8(wf2a, wf2b), xf[14], acc[0]); PIN(acc[0]); SBAR();
    wf1a = lds_tr(wa2 + 22528);
    wf1b = lds_tr(wa2 + 23040);
    SBAR();
    acc[0] = MFMA32(cat8(wf3a, wf3b), xf[15], acc[0]); PIN(acc[0]); SBAR();
    wf2a = lds_tr(wa2 + 24576);
    wf2b = lds_tr(wa2 + 25088);
    glds16i<122880>(sb1, vM, wave1k);
    SBAR();
    acc[1] = MFMA32(cat8(wf4a, wf4b), xf[12], acc[1]); PIN(acc[1]); SBAR();
    wf3a = lds_tr(wa2 + 26624);
    wf3b = lds_tr(wa2 + 27136);
    SBAR();
    acc[1] = MFMA32(cat8(wf5a, wf5b), xf[13], acc[1]); PIN(acc[1]); SBAR();
    wf4a = lds_tr(wa2 + 28672);
    wf4b = lds_tr(wa2 + 29184);
    glds16i<131072>(sb2, vM, wave1k); vM += stepM; PIN(vM);
    SBAR();
    acc[1] = MFMA32(cat8(wf0a, wf0b), xf[14], acc[1]); PIN(acc[1]); SBAR();
    wf5a = lds_tr(wa2 + 30720);
    wf5b = lds_tr(wa2 + 31232);
    SBAR();
    acc[1] = MFMA32(cat8(wf1a, wf1b), xf[15], acc[1]); PIN(acc[1]); SBAR();
    SBAR();
    waitv_bar<13>();
        acc[2] = MFMA32(cat8(wf2a, wf2b), xf[12], acc[2]); PIN(acc[2]); SBAR();
    wf0a = lds_tr(wa1 + 0);
    wf0b = lds_tr(wa1 + 512);
    xf[16] = lds_b128(xa0 + 0);
    SBAR();
    acc[2] = MFMA32(cat8(wf3a, wf3b), xf[13], acc[2]); PIN(acc[2]); SBAR();
    wf1a = lds_tr(wa1 + 2048);
    wf1b = lds_tr(wa1 + 2560);
    xf[17] = lds_b128(xa1 + 0);
    glds16i<49152>(xp, vx0, wave2k);
    SBAR();
    acc[2] = MFMA32(cat8(wf4a, wf4b), xf[14], acc[2]); PIN(acc[2]); SBAR();
    wf2a = lds_tr(wa1 + 4096);
    wf2b = lds_tr(wa1 + 4608);
    xf[18] = lds_b128(xa2 + 0);
    SBAR();
    acc[2] = MFMA32(cat8(wf5a, wf5b), xf[15], acc[2]); PIN(acc[2]); SBAR();
    wf3a = lds_tr(wa1 + 6144);
    wf3b = lds_tr(wa1 + 6656);
    xf[19] = lds_b128(xa3 + 0);
    glds16i<50176>(xp, vx1, wave2k);
    SBAR();
    acc[0] = MFMA32(cat8(wf0a, wf0b), xf[16], acc[0]); PIN(acc[0]); SBAR();
    wf4a = lds_tr(wa1 + 8192);
    wf4b = lds_tr(wa1 + 8704);
    wf5a = lds_tr(wa1 + 10240);
    wf5b = lds_tr(wa1 + 10752);
    SBAR();
    acc[0] = MFMA32(cat8(wf1a, wf1b), xf[17], acc[0]); PIN(acc[0]); SBAR();
    wf0a = lds_tr(wa1 + 12288);
    wf0b = lds_tr(wa1 + 12800);
    glds16i<139264>(sb0, v0, wave1k);
    SBAR();
    acc[0] = MFMA32(cat8(wf2a, wf2b), xf[18], acc[0]); PIN(acc[0]); SBAR();
    wf1a = lds_tr(wa1 + 14336);
    wf1b = lds_tr(wa1 + 14848);
    SBAR();
    acc[0] = MFMA32(cat8(wf3a, wf3b), xf[19], acc[0]); PIN(acc[0]); SBAR();
    wf2a = lds_tr(wa1 + 16384);
    wf2b = lds_tr(wa1 + 16896);
    glds16i<147456>(sb1, vM, wave1k);
    SBAR();
    acc[1] = MFMA32(cat8(wf4a, wf4b), xf[16], acc[1]); PIN(acc[1]); SBAR();
    wf3a = lds_tr(wa1 + 18432);
    wf3b = lds_tr(wa1 + 18944);
    SBAR();
    acc[1] = MFMA32(cat8(wf5a, wf5b), xf[17], acc[1]); PIN(acc[1]); SBAR();
    wf4a = lds_tr(wa1 + 20480);
    wf4b = lds_tr(wa1 + 20992);
    glds16i<155648>(sb2, vM, wave1k);
    SBAR();
    acc[1] = MFMA32(cat8(wf0a, wf0b), xf[18], acc[1]); PIN(acc[1]); SBAR();
    wf5a = lds_tr(wa1 + 22528);
    wf5b = lds_tr(wa1 + 23040);
    SBAR();
    acc[1] = MFMA32(cat8(wf1a, wf1b), xf[19], acc[1]); PIN(acc[1]); SBAR();
    SBAR();
    waitv_bar<10>();
    acc[2] = MFMA32(cat8(wf2a, wf2b), xf[16], acc[2]); PIN(acc[2]); SBAR();
    wf0a = lds_tr(wa1 + 24576);
    wf0b = lds_tr(wa1 + 25088);
    xf[20] = lds_b128(xa0 + 16384);
    T1LOAD;
    SBAR();
    acc[2] = MFMA32(cat8(wf3a, wf3b), xf[17], acc[2]); PIN(acc[2]); SBAR();
    wf1a = lds_tr(wa1 + 26624);
    wf1b = lds_tr(wa1 + 27136);
    xf[21] = lds_b128(xa1 + 16384);
    glds16i<65536>(sb3, vF, wave1k);
    SBAR();
    acc[2] = MFMA32(cat8(wf4a, wf4b), xf[18], acc[2]); PIN(acc[2]); SBAR();
    wf2a = lds_tr(wa1 + 28672);
    wf2b = lds_tr(wa1 + 29184);
    xf[22] = lds_b128(xa2 + 16384);
    SBAR();
    acc[2] = MFMA32(cat8(wf5a, wf5b), xf[19], acc[2]); PIN(acc[2]); SBAR();
    wf3a = lds_tr(wa1 + 30720);
    wf3b = lds_tr(wa1 + 31232);
    xf[23] = lds_b128(xa3 + 16384);
    glds16i<73728>(sb4, vF, wave1k); vF += 229376u; PIN(vF);
    SBAR();
    acc[0] = MFMA32(cat8(wf0a, wf0b), xf[20], acc[0]); PIN(acc[0]); SBAR();
    wf4a = lds_tr(wa1 + 32768);
    wf4b = lds_tr(wa1 + 33280);
    wf5a = lds_tr(wa1 + 34816);
    wf5b = lds_tr(wa1 + 35328);
    SBAR();
    acc[0] = MFMA32(cat8(wf1a, wf1b), xf[21], acc[0]); PIN(acc[0]); SBAR();
    wf0a = lds_tr(wa1 + 36864);
    wf0b = lds_tr(wa1 + 37376);
    glds16i<81920>(sb3, vF, wave1k);
    SBAR();
    acc[0] = MFMA32(cat8(wf2a, wf2b), xf[22], acc[0]); PIN(acc[0]); SBAR();
    wf1a = lds_tr(wa1 + 38912);
    wf1b = lds_tr(wa1 + 39424);
    SBAR();
    acc[0] = MFMA32(cat8(wf3a, wf3b), xf[23], acc[0]); PIN(acc[0]); SBAR();
    wf2a = lds_tr(wa1 + 40960);
    wf2b = lds_tr(wa1 + 41472);
    glds16i<0>(sb4, vF, wave1k); vF += rot == 6 ? 0u - 7 * 229376u : 229376u; PIN(vF);
    SBAR();
    acc[1] = MFMA32(cat8(wf4a, wf4b), xf[20], acc[1]); PIN(acc[1]); SBAR();
    wf3a = lds_tr(wa1 + 43008);
    wf3b = lds_tr(wa1 + 43520);
    TOUCHALL;
    SBAR();
    acc[1] = MFMA32(cat8(wf5a, wf5b), xf[21], acc[1]); PIN(acc[1]); SBAR();
    wf4a = lds_tr(wa1 + 45056);
    wf4b = lds_tr(wa1 + 45568);
    SBAR();
    acc[1] = MFMA32(cat8(wf0a, wf0b), xf[22], acc[1]); PIN(acc[1]); SBAR();
    wf5a = lds_tr(wa1 + 47104);
    wf5b = lds_tr(wa1 + 47616);
    SBAR();
    acc[1] = MFMA32(cat8(wf1a, wf1b), xf[23], acc[1]); PIN(acc[1]); SBAR();
    SBAR();
    waitv_bar<5>();
        acc[2] = MFMA32(cat8(wf2a, wf2b), xf[20], acc[2]); PIN(acc[2]); SBAR();
    wf0a = lds_tr(wa1 + 49152);
    wf0b = lds_tr(wa1 + 49664);
    xf[24] = lds_b128(xa0 + 32768);
    T1READ;
    glds16i<90112>(sb3, vF, wave1k);
    SBAR();
    acc[2] = MFMA32(cat8(wf3a, wf3b), xf[21], acc[2]); PIN(acc[2]); SBAR();
    wf1a = lds_tr(wa1 + 51200);
    wf1b = lds_tr(wa1 + 51712);
    xf[25] = lds_b128(xa1 + 32768);
    SBAR();
    acc[2] = MFMA32(cat8(wf4a, wf4b), xf[22], acc[2]); PIN(acc[2]); SBAR();
    wf2a = lds_tr(wa1 + 53248);
    wf2b = lds_tr(wa1 + 53760);
    xf[26] = lds_b128(xa2 + 32768);
    BAD1;
    glds16i<16384>(sb4, vF, wave1k); vF += 229376u; PIN(vF);
    SBAR();
    acc[2] = MFMA32(cat8(wf5a, wf5b), xf[23], acc[2]); PIN(acc[2]); SBAR();
    wf3a = lds_tr(wa1 + 55296);
    wf3b = lds_tr(wa1 + 55808);
    xf[27] = lds_b128(xa3 + 32768);
    CHECK1;
    SBAR();
    acc[0] = MFMA32(cat8(wf0a, wf0b), xf[24], acc[0]); PIN(acc[0]); SBAR();
    wf4a = lds_tr(wa1 + 57344);
    wf4b = lds_tr(wa1 + 57856);
    wf5a = lds_tr(wa1 + 59392);
    wf5b = lds_tr(wa1 + 59904);
    STATS;
    glds16i<24576>(sb3, vF, wave1k);
    SBAR();
    acc[0] = MFMA32(cat8(wf1a, wf1b), xf[25], acc[0]); PIN(acc[0]); SBAR();
    wf0a = lds_tr(wa1 + 61440);
    wf0b = lds_tr(wa1 + 61952);
    ca0 = CDUP(ca0); PIN(ca0);
    SBAR();
    acc[0] = MFMA32(ca0, MEAN, acc[0]); PIN(acc[0]); SBAR();
    acc[0] = MFMA32(cat8(wf2a, wf2b), xf[26], acc[0]); PIN(acc[0]); SBAR();
    wf1a = lds_tr(wa1 + 63488);
    wf1b = lds_tr(wa1 + 64000);
    glds16i<98304>(sb4, vF, wave1k); vF += rot == 4 ? 0u - 7 * 229376u : 229376u; PIN(vF);
    SBAR();
    acc[0] = MFMA32(cat8(wf3a, wf3b), xf[27], acc[0]); PIN(acc[0]); SBAR();
    wf2a = lds_tr(wa2 + 0);
    wf2b = lds_tr(wa2 + 512);
    ca1 = CDUP(ca1); PIN(ca1);
    SBAR();
    acc[1] = MFMA32(ca1, MEAN, acc[1]); PIN(acc[1]); SBAR();
    acc[1] = MFMA32(cat8(wf4a, wf4b), xf[24], acc[1]); PIN(acc[1]); SBAR();
    wf3a = lds_tr(wa2 + 2048);
    wf3b = lds_tr(wa2 + 2560);
    vU = vF; glds16i<106496>(sb3, vF, wave1k); vF += 229376u; PIN(vF);
    SBAR();
    acc[1] = MFMA32(cat8(wf5a, wf5b), xf[25], acc[1]); PIN(acc[1]); SBAR();
    wf4a = lds_tr(wa2 + 4096);
    wf4b = lds_tr(wa2 + 4608);
    ca2 = CDUP(ca2); PIN(ca2);
    SBAR();
    acc[2] = MFMA32(ca2, MEAN, acc[2]); PIN(acc[2]); SBAR();
    acc[1] = MFMA32(cat8(wf0a, wf0b), xf[26], acc[1]); PIN(acc[1]); SBAR();
    wf5a = lds_tr(wa2 + 6144);
    wf5b = lds_tr(wa2 + 6656);
    SBAR();
    acc[1] = MFMA32(cat8(wf1a, wf1b), xf[27], acc[1]); PIN(acc[1]); SBAR();
    SBAR();
    waitv_bar<6>();
    acc[2] = MFMA32(cat8(wf2a, wf2b), xf[24], acc[2]); PIN(acc[2]); SBAR();
    wf0a = lds_tr(wa2 + 8192);
    wf0b = lds_tr(wa2 + 8704);
    xf[28] = lds_b128(xa0 + 49152);
    cf0a = lds_tr(wc2a + 0);
    cf0b = lds_tr(wc2a + 0 + 512);
    glds16i<114688>(sb3, vF, wave1k); vF += rot == 2 ? 0u - 7 * 229376u : 229376u; PIN(vF);
    SBAR();
    acc[2] = MFMA32(cat8(wf3a, wf3b), xf[25], acc[2]); PIN(acc[2]); SBAR();
    wf1a = lds_tr(wa2 + 10240);
    wf1b = lds_tr(wa2 + 10752);
    xf[29] = lds_b128(xa1 + 49152);
    cf1a = lds_tr(wc2a + 2048);
    cf1b = lds_tr(wc2a + 2048 + 512);
    SBAR();
    acc[2] = MFMA32(cat8(wf4a, wf4b), xf[26], acc[2]); PIN(acc[2]); SBAR();
    wf2a = lds_tr(wa2 + 12288);
    wf2b = lds_tr(wa2 + 12800);
    xf[30] = lds_b128(xa2 + 49152);
    cf2a = lds_tr(wc2a + 4096);
    cf2b = lds_tr(wc2a + 4096 + 512);
    glds16i<122880>(sb3, vF, wave1k); vF += 229376u; PIN(vF);
    SBAR();
    cs = MFMA16(cat8(cf0a, cf0b), cpat, zero4); PIN(cs); SBAR();
    acc[2] = MFMA32(cat8(wf5a, wf5b), xf[27], acc[2]); PIN(acc[2]); SBAR();
    wf3a = lds_tr(wa2 + 14336);
    wf3b = lds_tr(wa2 + 14848);
    xf[31] = lds_b128(xa3 + 49152);
    cf0a = lds_tr(wc2a + 6144);
    cf0b = lds_tr(wc2a + 6144 + 512);
    SBAR();
    cs = MFMA16(cat8(cf1a, cf1b), cpat, cs); PIN(cs); SBAR();
    acc[0] = MFMA32(cat8(wf0a, wf0b), xf[28], acc[0]); PIN(acc[0]); SBAR();
    wf4a = lds_tr(wa2 + 16384);
    wf4b = lds_tr(wa2 + 16896);
    wf5a = lds_tr(wa2 + 18432);
    wf5b = lds_tr(wa2 + 18944);
    PIN(lane16); unsigned vsrc2 = (lane16 >> 7) * 1024 + ((lane16 & 112u) ^ ((lane16 >> 2) & 64u)); PIN(vsrc2);
    glds16i<8192>(sb3, vF, wave1k);
    SBAR();
    cs = MFMA16(cat8(cf2a, cf2b), cpat, cs); PIN(cs); SBAR();
    acc[0] = MFMA32(cat8(wf1a, wf1b), xf[29], acc[0]); PIN(acc[0]); SBAR();
    wf0a = lds_tr(wa2 + 20480);
    wf0b = lds_tr(wa2 + 20992);
    cf1a = lds_tr(wc2b + 0);
    cf1b = lds_tr(wc2b + 0 + 512);
    PIN(lane16); unsigned vk0 = tg > 0 ? lane16 : lane16 & 512u, vv = (lane16 & 16u) * 32 + (lane16 >> 5) * 16; PIN2(vk0, vv);
    SBAR();
    cs = MFMA16(cat8(cf0a, cf0b), cpat, cs); PIN(cs); SBAR();
    acc[0] = MFMA32(cat8(wf2a, wf2b), xf[30], acc[0]); PIN(acc[0]); SBAR();
    wf1a = lds_tr(wa2 + 22528);
    wf1b = lds_tr(wa2 + 23040);
    cf2a = lds_tr(wc2b + 2048);
    cf2b = lds_tr(wc2b + 2048 + 512);
    PIN(lane16); int sel = lane16 < 80 ? lane16 >> 4 : 0, fu_ = (sel == 0 ? 0 : 16 - 8 * (sel & 1)) + (head >> 2); PIN2(sel, fu_);
    glds16i<32768>(sb4, vU, wave1k); vU += 229376u; PIN(vU);
    SBAR();
    acc[0] = MFMA32(cat8(wf3a, wf3b), xf[31], acc[0]); PIN(acc[0]); SBAR();
    wf2a = lds_tr(wa2 + 24576);
    wf2b = lds_tr(wa2 + 25088);
    cf0a = lds_tr(wc2b + 4096);
    cf0b = lds_tr(wc2b + 4096 + 512);
    int fT = sel >= 3 ? (tg > 0 ? qt - 8 : 0) : qt, fq_ = (fu_ * 43) >> 8; PIN2(fT, fq_);
    SBAR();
    cs = MFMA16(cat8(cf1a, cf1b), cpat, cs); PIN(cs); SBAR();
    acc[1] = MFMA32(cat8(wf4a, wf4b), xf[28], acc[1]); PIN(acc[1]); SBAR();
    wf3a = lds_tr(wa2 + 26624);
    wf3b = lds_tr(wa2 + 27136);
    cf1a = lds_tr(wc2b + 6144);
    cf1b = lds_tr(wc2b + 6144 + 512);
    fu_ = min(fu_ - 6 * fq_, 3) + 4 * fq_; PIN(fu_);
    glds16i<40960>(sb4, vU, wave1k); vU += rot == 2 ? 0u - 7 * 229376u : 229376u; PIN(vU);
    SBAR();
    cs = MFMA16(cat8(cf2a, cf2b), cpat, cs); PIN(cs); SBAR();
    acc[1] = MFMA32(cat8(wf5a, wf5b), xf[29], acc[1]); PIN(acc[1]); SBAR();
    wf4a = lds_tr(wa2 + 28672);
    wf4b = lds_tr(wa2 + 29184);
    unsigned fq_off = WS_FP + (((fT >> 2) * 16 + fu_) * 8 + ((head >> 1) & 1) * 4 + (fT & 3)) * 128; PIN(fq_off);
    asm volatile("v_mul_f32 %0, %3, %4\n\tv_mul_f32 %1, %3, %5\n\tv_cvt_pk_bf16_f32 %2, %0, %1\n\tv_mul_f32 %0, %3, %6" : "=&v"(t0), "=&v"(t1), "=&v"(hp0) : "v"(qs), "v"(acc[0][0]), "v"(acc[0][1]), "v"(acc[0][2]));
    SBAR();
    cs = MFMA16(cat8(cf0a, cf0b), cpat, cs); PIN(cs); SBAR();
    acc[1] = MFMA32(cat8(wf0a, wf0b), xf[30], acc[1]); PIN(acc[1]); SBAR();
    wf5a = lds_tr(wa2 + 30720);
    wf5b = lds_tr(wa2 + 31232);
    asm volatile("v_mul_f32 %0, %3, %4\n\tv_cvt_pk_bf16_f32 %2, %1, %0\n\tv_mul_f32 %1, %3, %5\n\tv_mul_f32 %0, %3, %6" : "=&v"(t1), "+v"(t0), "=&v"(hp1) : "v"(qs), "v"(acc[0][3]), "v"(acc[0][4]), "v"(acc[0][5]));
    SBAR();
    cs = MFMA16(cat8(cf1a, cf1b), cpat, cs); PIN(cs); SBAR();
    acc[1] = MFMA32(cat8(wf1a, wf1b), xf[31], acc[1]); PIN(acc[1]); SBAR();
    wf0a = lds_tr(wa1 + 0);
    wf0b = lds_tr(wa1 + 512);
    asm volatile("v_cvt_pk_bf16_f32 %2, %0, %1\n\tv_mul_f32 %0, %4, %5\n\tv_mul_f32 %1, %4, %6\n\tv_cvt_pk_bf16_f32 %3, %0, %1" : "+v"(t0), "+v"(t1), "=&v"(hp2), "=&v"(hp3) : "v"(qs), "v"(acc[0][6]), "v"(acc[0][7]));
    SBAR();
        acc[2] = MFMA32(cat8(wf2a, wf2b), xf[28], acc[2]); PIN(acc[2]); SBAR();
    wf1a = lds_tr(wa1 + 2048);
    wf1b = lds_tr(wa1 + 2560);
    gstore16_dev(spin(qdst) + 0, lane16, u32x4{hp0, hp1, hp2, hp3});
    asm volatile("v_mul_f32 %0, %3, %4\n\tv_mul_f32 %1, %3, %5\n\tv_cvt_pk_bf16_f32 %2, %0, %1" : "=&v"(t0), "=&v"(t1), "=&v"(hp0) : "v"(qs), "v"(acc[0][8]), "v"(acc[0][9]));
    SBAR();
    acc[2] = MFMA32(cat8(wf3a, wf3b), xf[29], acc[2]); PIN(acc[2]); SBAR();
    wf2a = lds_tr(wa1 + 4096);
    wf2b = lds_tr(wa1 + 4608);
    CSPACK;
    asm volatile("v_mul_f32 %0, %3, %4\n\tv_mul_f32 %1, %3, %5\n\tv_cvt_pk_bf16_f32 %2, %0, %1\n\tv_mul_f32 %0, %3, %6" : "=&v"(t0), "=&v"(t1), "=&v"(hp1) : "v"(qs), "v"(acc[0][10]), "v"(acc[0][11]), "v"(acc[0][12]));
    SBAR();
    acc[2] = MFMA32(cat8(wf4a, wf4b), xf[30], acc[2]); PIN(acc[2]); SBAR();
    wf3a = lds_tr(wa1 + 6144);
    wf3b = lds_tr(wa1 + 6656);
    asm volatile("v_mul_f32 %0, %3, %4\n\tv_cvt_pk_bf16_f32 %2, %1, %0\n\tv_mul_f32 %1, %3, %5\n\tv_mul_f32 %0, %3, %6" : "=&v"(t1), "+v"(t0), "=&v"(hp2) : "v"(qs), "v"(acc[0][13]), "v"(acc[0][14]), "v"(acc[0][15]));
    CSSTORE(cs2dst, tq < 2);
    SBAR();
    acc[2] = MFMA32(cat8(wf5a, wf5b), xf[31], acc[2]); PIN(acc[2]); SBAR();
    wf4a = lds_tr(wa1 + 8192);
    wf4b = lds_tr(wa1 + 8704);
    asm volatile("v_cvt_pk_bf16_f32 %0, %1, %2" : "=&v"(hp3) : "v"(t0), "v"(t1));
    gstore16_dev(spin(qdst) + 65536, lane16, u32x4{hp0, hp1, hp2, hp3});
    SBAR();
    asm volatile("" : "+s"(tsel));
    if (tsel != 0) {
        acc3 = MFMA32(cat8(wf0a, wf0b), xf[0], zero16); PIN(acc3); SBAR();
        wf5a = lds_tr(wa1 + 10240);
        wf5b = lds_tr(wa1 + 10752);
        asm volatile("v_mul_f32 %0, %3, %4\n\tv_mul_f32 %1, %3, %5\n\tv_cvt_pk_bf16_f32 %2, %0, %1\n\tv_mul_f32 %0, %3, %6\n\tv_mul_f32 %1, %3, %7" : "=&v"(t0), "=&v"(t1), "=&v"(hp0) : "v"(qs), "v"(acc[1][0]), "v"(acc[1][1]), "v"(acc[1][2]), "v"(acc[1][3]));
        SBAR();
        acc3 = MFMA32(cat8(wf1a, wf1b), xf[1], acc3); PIN(acc3); SBAR();
        wf0a = lds_tr(wa1 + 12288);
        wf0b = lds_tr(wa1 + 12800);
        asm volatile("v_cvt_pk_bf16_f32 %2, %0, %1\n\tv_mul_f32 %0, %4, %5\n\tv_mul_f32 %1, %4, %6\n\tv_cvt_pk_bf16_f32 %3, %0, %1\n\tv_mul_f32 %0, %4, %7" : "+v"(t0), "+v"(t1), "=&v"(hp1), "=&v"(hp2) : "v"(qs), "v"(acc[1][4]), "v"(acc[1][5]), "v"(acc[1][6]));
        SBAR();
        acc3 = MFMA32(cat8(wf2a, wf2b), xf[2], acc3); PIN(acc3); SBAR();
        wf1a = lds_tr(wa1 + 14336);
        wf1b = lds_tr(wa1 + 14848);
        asm volatile("v_mul_f32 %0, %2, %3\n\tv_cvt_pk_bf16_f32 %1, %4, %0" : "=&v"(t1), "=&v"(hp3) : "v"(qs), "v"(acc[1][7]), "v"(t0));
        gstore16_dev(spin(qdst) + 262144, lane16, u32x4{hp0, hp1, hp2, hp3});
        asm volatile("v_mul_f32 %0, %2, %3\n\tv_mul_f32 %1, %2, %4" : "=&v"(t0), "=&v"(t1) : "v"(qs), "v"(acc[1][8]), "v"(acc[1][9]));
        SBAR();
        acc3 = MFMA32(cat8(wf3a, wf3b), xf[3], acc3); PIN(acc3); SBAR();
        wf2a = lds_tr(wa1 + 16384);
        wf2b = lds_tr(wa1 + 16896);
        asm volatile("v_cvt_pk_bf16_f32 %2, %0, %1\n\tv_mul_f32 %0, %4, %5\n\tv_mul_f32 %1, %4, %6\n\tv_cvt_pk_bf16_f32 %3, %0, %1\n\tv_mul_f32 %0, %4, %7" : "+v"(t0), "+v"(t1), "=&v"(hp0), "=&v"(hp1) : "v"(qs), "v"(acc[1][10]), "v"(acc[1][11]), "v"(acc[1][12]));
        SBAR();
        acc4 = MFMA32(cat8(wf4a, wf4b), xf[0], zero16); PIN(acc4); SBAR();
        wf3a = lds_tr(wa1 + 18432);
        wf3b = lds_tr(wa1 + 18944);
        asm volatile("v_mul_f32 %0, %4, %5\n\tv_cvt_pk_bf16_f32 %2, %1, %0\n\tv_mul_f32 %1, %4, %6\n\tv_mul_f32 %0, %4, %7\n\tv_cvt_pk_bf16_f32 %3, %1, %0" : "=&v"(t1), "+v"(t0), "=&v"(hp2), "=&v"(hp3) : "v"(qs), "v"(acc[1][13]), "v"(acc[1][14]), "v"(acc[1][15]));
        SBAR();
        acc4 = MFMA32(cat8(wf5a, wf5b), xf[1], acc4); PIN(acc4); SBAR();
        wf4a = lds_tr(wa1 + 20480);
        wf4b = lds_tr(wa1 + 20992);
        gstore16_dev(spin(qdst) + 327680, lane16, u32x4{hp0, hp1, hp2, hp3});
        SBAR();
        acc4 = MFMA32(cat8(wf0a, wf0b), xf[2], acc4); PIN(acc4); SBAR();
        wf5a = lds_tr(wa1 + 22528);
        wf5b = lds_tr(wa1 + 23040);
        asm volatile("v_mul_f32 %0, %3, %4\n\tv_mul_f32 %1, %3, %5\n\tv_cvt_pk_bf16_f32 %2, %0, %1\n\tv_mul_f32 %0, %3, %6\n\tv_mul_f32 %1, %3, %7" : "=&v"(t0), "=&v"(t1), "=&v"(hp0) : "v"(qs), "v"(acc[2][0]), "v"(acc[2][1]), "v"(acc[2][2]), "v"(acc[2][3]));
        SBAR();
        acc4 = MFMA32(cat8(wf1a, wf1b), xf[3], acc4); PIN(acc4); SBAR();
        wf0a = lds_tr(wa0 + 0);
        wf0b = lds_tr(wa0 + 512);
        asm volatile("v_cvt_pk_bf16_f32 %2, %0, %1\n\tv_mul_f32 %0, %4, %5\n\tv_mul_f32 %1, %4, %6\n\tv_cvt_pk_bf16_f32 %3, %0, %1\n\tv_mul_f32 %0, %4, %7" : "+v"(t0), "+v"(t1), "=&v"(hp1), "=&v"(hp2) : "v"(qs), "v"(acc[2][4]), "v"(acc[2][5]), "v"(acc[2][6]));
        SBAR();
        acc3 = MFMA32(cat8(wf2a, wf2b), xf[4], acc3); PIN(acc3); SBAR();
        wf1a = lds_tr(wa0 + 2048);
        wf1b = lds_tr(wa0 + 2560);
        asm volatile("v_mul_f32 %0, %2, %3\n\tv_cvt_pk_bf16_f32 %1, %4, %0" : "=&v"(t1), "=&v"(hp3) : "v"(qs), "v"(acc[2][7]), "v"(t0));
        gstore16_dev(spin(qdst) + 524288, lane16, u32x4{hp0, hp1, hp2, hp3});
        asm volatile("v_mul_f32 %0, %2, %3\n\tv_mul_f32 %1, %2, %4" : "=&v"(t0), "=&v"(t1) : "v"(qs), "v"(acc[2][8]), "v"(acc[2][9]));
        SBAR();
        acc3 = MFMA32(cat8(wf3a, wf3b), xf[5], acc3); PIN(acc3); SBAR();
        wf2a = lds_tr(wa0 + 4096);
        wf2b = lds_tr(wa0 + 4608);
        asm volatile("v_cvt_pk_bf16_f32 %2, %0, %1\n\tv_mul_f32 %0, %4, %5\n\tv_mul_f32 %1, %4, %6\n\tv_cvt_pk_bf16_f32 %3, %0, %1\n\tv_mul_f32 %0, %4, %7" : "+v"(t0), "+v"(t1), "=&v"(hp0), "=&v"(hp1) : "v"(qs), "v"(acc[2][10]), "v"(acc[2][11]), "v"(acc[2][12]));
        SBAR();
        acc3 = MFMA32(cat8(wf4a, wf4b), xf[6], acc3); PIN(acc3); SBAR();
        wf3a = lds_tr(wa0 + 6144);
        wf3b = lds_tr(wa0 + 6656);
        asm volatile("v_mul_f32 %0, %4, %5\n\tv_cvt_pk_bf16_f32 %2, %1, %0\n\tv_mul_f32 %1, %4, %6\n\tv_mul_f32 %0, %4, %7\n\tv_cvt_pk_bf16_f32 %3, %1, %0" : "=&v"(t1), "+v"(t0), "=&v"(hp2), "=&v"(hp3) : "v"(qs), "v"(acc[2][13]), "v"(acc[2][14]), "v"(acc[2][15]));
        SBAR();
        acc3 = MFMA32(cat8(wf5a, wf5b), xf[7], acc3); PIN(acc3); SBAR();
        gstore16_dev(spin(qdst) + 589824, lane16, u32x4{hp0, hp1, hp2, hp3});
        SBAR();
        waitv_bar<13>();
        acc4 = MFMA32(cat8(wf0a, wf0b), xf[4], acc4); PIN(acc4); SBAR();
        wf4a = lds_tr(wa1 + 24576);
        wf4b = lds_tr(wa1 + 25088);
        wf5a = lds_tr(wa1 + 26624);
        wf5b = lds_tr(wa1 + 27136);
        SBAR();
        acc4 = MFMA32(cat8(wf1a, wf1b), xf[5], acc4); PIN(acc4); SBAR();
        wf0a = lds_tr(wa1 + 28672);
        wf0b = lds_tr(wa1 + 29184);
        SBAR();
        acc4 = MFMA32(cat8(wf2a, wf2b), xf[6], acc4); PIN(acc4); SBAR();
        wf1a = lds_tr(wa1 + 30720);
        wf1b = lds_tr(wa1 + 31232);
        SBAR();
        acc4 = MFMA32(cat8(wf3a, wf3b), xf[7], acc4); PIN(acc4); SBAR();
        wf2a = lds_tr(wa0 + 16384);
        wf2b = lds_tr(wa0 + 16896);
        SBAR();
        acc3 = MFMA32(cat8(wf4a, wf4b), xf[8], acc3); PIN(acc3); SBAR();
        wf3a = lds_tr(wa0 + 18432);
        wf3b = lds_tr(wa0 + 18944);
        glds16i<49152>(sb4, vU, wave1k); vU += 229376u; PIN(vU);
        SBAR();
        acc3 = MFMA32(cat8(wf5a, wf5b), xf[9], acc3); PIN(acc3); SBAR();
        wf4a = lds_tr(wa0 + 20480);
        wf4b = lds_tr(wa0 + 20992);
        SBAR();
        acc3 = MFMA32(cat8(wf0a, wf0b), xf[10], acc3); PIN(acc3); SBAR();
        wf5a = lds_tr(wa0 + 22528);
        wf5b = lds_tr(wa0 + 23040);
        SBAR();
        acc3 = MFMA32(cat8(wf1a, wf1b), xf[11], acc3); PIN(acc3); SBAR();
        wf0a = lds_tr(wa0 + 24576);
        wf0b = lds_tr(wa0 + 25088);
        SBAR();
        acc4 = MFMA32(cat8(wf2a, wf2b), xf[8], acc4); PIN(acc4); SBAR();
        wf1a = lds_tr(wa0 + 26624);
        wf1b = lds_tr(wa0 + 27136);
        SBAR();
        acc4 = MFMA32(cat8(wf3a, wf3b), xf[9], acc4); PIN(acc4); SBAR();
        wf2a = lds_tr(wa0 + 28672);
        wf2b = lds_tr(wa0 + 29184);
        SBAR();
        acc4 = MFMA32(cat8(wf4a, wf4b), xf[10], acc4); PIN(acc4); SBAR();
        wf3a = lds_tr(wa0 + 30720);
        wf3b = lds_tr(wa0 + 31232);
        glds16i<57344>(sb4, vU, wave1k);
        SBAR();
        acc4 = MFMA32(cat8(wf5a, wf5b), xf[11], acc4); PIN(acc4); SBAR();
        wf4a = lds_tr(wa1 + 32768);
        wf4b = lds_tr(wa1 + 33280);
        SBAR();
        waitv<2>();
        acc3 = MFMA32(cat8(wf0a, wf0b), xf[12], acc3); PIN(acc3); SBAR();
        wf5a = lds_tr(wa1 + 34816);
        wf5b = lds_tr(wa1 + 35328);
        flag_store(ws, WS_FP + myflag, tagv);
        glds16i<G2_SA + 0>(spin(wd) + (UNIT(0) * 65536 + 0), vsrc2, wave4k);
        SBAR();
        acc3 = MFMA32(cat8(wf1a, wf1b), xf[13], acc3); PIN(acc3); SBAR();
        wf0a = lds_tr(wa1 + 36864);
        wf0b = lds_tr(wa1 + 37376);
        SBAR();
        acc3 = MFMA32(cat8(wf2a, wf2b), xf[14], acc3); PIN(acc3); SBAR();
        wf1a = lds_tr(wa1 + 38912);
        wf1b = lds_tr(wa1 + 39424);
        glds16i<G2_SA + 1024>(spin(wd) + (UNIT(0) * 65536 + 8192), vsrc2, wave4k);
        SBAR();
        acc3 = MFMA32(cat8(wf3a, wf3b), xf[15], acc3); PIN(acc3); SBAR();
        SBAR();
        waitv_bar<14>();
                acc4 = MFMA32(cat8(wf4a, wf4b), xf[12], acc4); PIN(acc4); SBAR();
        wf2a = lds_tr(wa1 + 40960);
        wf2b = lds_tr(wa1 + 41472);
        wf3a = lds_tr(wa1 + 43008);
        wf3b = lds_tr(wa1 + 43520);
        glds16i<G2_SA + 2048>(spin(wd) + (UNIT(0) * 65536 + 16384), vsrc2, wave4k);
        SBAR();
        acc4 = MFMA32(cat8(wf5a, wf5b), xf[13], acc4); PIN(acc4); SBAR();
        wf4a = lds_tr(wa1 + 45056);
        wf4b = lds_tr(wa1 + 45568);
        T2LOAD;
        SBAR();
        acc4 = MFMA32(cat8(wf0a, wf0b), xf[14], acc4); PIN(acc4); SBAR();
        wf5a = lds_tr(wa1 + 47104);
        wf5b = lds_tr(wa1 + 47616);
        SBAR();
        acc4 = MFMA32(cat8(wf1a, wf1b), xf[15], acc4); PIN(acc4); SBAR();
        wf0a = lds_tr(wa1 + 49152);
        wf0b = lds_tr(wa1 + 49664);
        glds16i<G2_SA + 3072>(spin(wd) + (UNIT(0) * 65536 + 24576), vsrc2, wave4k);
        SBAR();
        acc3 = MFMA32(cat8(wf2a, wf2b), xf[16], acc3); PIN(acc3); SBAR();
        wf1a = lds_tr(wa1 + 51200);
        wf1b = lds_tr(wa1 + 51712);
        SBAR();
        acc3 = MFMA32(cat8(wf3a, wf3b), xf[17], acc3); PIN(acc3); SBAR();
        wf2a = lds_tr(wa1 + 53248);
        wf2b = lds_tr(wa1 + 53760);
        glds16i<G2_SB + 0>(spin(wd) + (UNIT(1) * 65536 + 0), vsrc2, wave4k);
        SBAR();
        acc3 = MFMA32(cat8(wf4a, wf4b), xf[18], acc3); PIN(acc3); SBAR();
        wf3a = lds_tr(wa1 + 55296);
        wf3b = lds_tr(wa1 + 55808);
        SBAR();
        acc3 = MFMA32(cat8(wf5a, wf5b), xf[19], acc3); PIN(acc3); SBAR();
        wf4a = lds_tr(wa1 + 57344);
        wf4b = lds_tr(wa1 + 57856);
        SBAR();
        acc3 = MFMA32(cat8(wf0a, wf0b), xf[20], acc3); PIN(acc3); SBAR();
        wf5a = lds_tr(wa1 + 59392);
        wf5b = lds_tr(wa1 + 59904);
        glds16i<G2_SB + 1024>(spin(wd) + (UNIT(1) * 65536 + 8192), vsrc2, wave4k);
        SBAR();
        acc3 = MFMA32(cat8(wf1a, wf1b), xf[21], acc3); PIN(acc3); SBAR();
        wf0a = lds_tr(wa1 + 61440);
        wf0b = lds_tr(wa1 + 61952);
        SBAR();
        UNDEF(acc[1]); UNDEF(acc[2]); UNDEF(qs);
        UNDEF(xf[0]); UNDEF(xf[1]); UNDEF(xf[2]); UNDEF(xf[3]); UNDEF(xf[4]); UNDEF(xf[5]); UNDEF(xf[6]); UNDEF(xf[7]); UNDEF(xf[8]); UNDEF(xf[9]); UNDEF(xf[10]); UNDEF(xf[11]); UNDEF(xf[12]); UNDEF(xf[13]); UNDEF(xf[14]); UNDEF(xf[15]);
    }
    asm volatile("" : "+s"(tsel));
    if (tsel == 0) {
        acc3 = MFMA32(cat8(wf0a, wf0b), xf[0], zero16); PIN(acc3); SBAR();
        wf5a = lds_tr(wa1 + 10240);
        wf5b = lds_tr(wa1 + 10752);
        SBAR();
        acc3 = MFMA32(cat8(wf1a, wf1b), xf[1], acc3); PIN(acc3); SBAR();
        wf0a = lds_tr(wa1 + 12288);
        wf0b = lds_tr(wa1 + 12800);
        asm volatile("v_mul_f32 %0, %2, %2\n\tv_mul_f32 %1, %3, %3\n\tv_fma_f32 %0, %0, %4, %5\n\tv_fma_f32 %1, %1, %4, %5\n\tv_mul_f32 %0, %0, %2" : "=&v"(t0), "=&v"(t1) : "v"(acc[1][0]), "v"(acc[1][1]), "v"(gk2), "v"(gk1));
        SBAR();
        acc3 = MFMA32(cat8(wf2a, wf2b), xf[2], acc3); PIN(acc3); SBAR();
        wf1a = lds_tr(wa1 + 14336);
        wf1b = lds_tr(wa1 + 14848);
        asm volatile("v_mul_f32 %0, %0, %2\n\tv_exp_f32 %1, %1\n\tv_exp_f32 %0, %0" : "+v"(t1), "+v"(t0) : "v"(acc[1][1]));
        SBAR();
        acc3 = MFMA32(cat8(wf3a, wf3b), xf[3], acc3); PIN(acc3); SBAR();
        wf2a = lds_tr(wa1 + 16384);
        wf2b = lds_tr(wa1 + 16896);
        asm volatile("v_mul_f32 %0, %4, %5\n\tv_mul_f32 %1, %6, %7\n\tv_fma_f32 %2, %2, %8, %8\n\tv_fma_f32 %3, %3, %8, %8" : "=&v"(p0), "=&v"(p1), "+v"(t0), "+v"(t1) : "v"(acc[1][0]), "v"(acc[2][0]), "v"(acc[1][1]), "v"(acc[2][1]), "v"(gvar));
        SBAR();
        acc4 = MFMA32(cat8(wf4a, wf4b), xf[0], zero16); PIN(acc4); SBAR();
        wf3a = lds_tr(wa1 + 18432);
        wf3b = lds_tr(wa1 + 18944);
        asm volatile("v_rcp_f32 %0, %0\n\tv_rcp_f32 %1, %1\n\tv_mul_f32 %2, %2, %0" : "+v"(t0), "+v"(t1), "+v"(p0) : );
        SBAR();
        acc4 = MFMA32(cat8(wf5a, wf5b), xf[1], acc4); PIN(acc4); SBAR();
        wf4a = lds_tr(wa1 + 20480);
        wf4b = lds_tr(wa1 + 20992);
        asm volatile("v_mul_f32 %0, %0, %1\n\tv_cvt_pk_bf16_f32 %2, %4, %0\n\tv_mul_f32 %3, %5, %5\n\tv_mul_f32 %1, %6, %6\n\tv_fma_f32 %3, %3, %7, %8" : "+v"(p1), "+v"(t1), "=&v"(hp0), "=&v"(t0) : "v"(p0), "v"(acc[1][2]), "v"(acc[1][3]), "v"(gk2), "v"(gk1));
        SBAR();
        acc4 = MFMA32(cat8(wf0a, wf0b), xf[2], acc4); PIN(acc4); SBAR();
        wf5a = lds_tr(wa1 + 22528);
        wf5b = lds_tr(wa1 + 23040);
        asm volatile("v_fma_f32 %0, %0, %2, %3\n\tv_mul_f32 %1, %1, %4\n\tv_mul_f32 %0, %0, %5\n\tv_exp_f32 %1, %1" : "+v"(t1), "+v"(t0) : "v"(gk2), "v"(gk1), "v"(acc[1][2]), "v"(acc[1][3]));
        SBAR();
        acc4 = MFMA32(cat8(wf1a, wf1b), xf[3], acc4); PIN(acc4); SBAR();
        wf0a = lds_tr(wa0 + 0);
        wf0b = lds_tr(wa0 + 512);
        asm volatile("v_exp_f32 %0, %0\n\tv_mul_f32 %1, %4, %5\n\tv_mul_f32 %2, %6, %7\n\tv_fma_f32 %3, %3, %8, %8" : "+v"(t1), "=&v"(p0), "=&v"(p1), "+v"(t0) : "v"(acc[1][2]), "v"(acc[2][2]), "v"(acc[1][3]), "v"(acc[2][3]), "v"(gvar));
        SBAR();
        acc3 = MFMA32(cat8(wf2a, wf2b), xf[4], acc3); PIN(acc3); SBAR();
        wf1a = lds_tr(wa0 + 2048);
        wf1b = lds_tr(wa0 + 2560);
        asm volatile("v_fma_f32 %0, %0, %2, %2\n\tv_rcp_f32 %1, %1\n\tv_rcp_f32 %0, %0" : "+v"(t1), "+v"(t0) : "v"(gvar));
        SBAR();
        acc3 = MFMA32(cat8(wf3a, wf3b), xf[5], acc3); PIN(acc3); SBAR();
        wf2a = lds_tr(wa0 + 4096);
        wf2b = lds_tr(wa0 + 4608);
        asm volatile("v_mul_f32 %0, %0, %1\n\tv_mul_f32 %2, %2, %3\n\tv_cvt_pk_bf16_f32 %4, %0, %2\n\tv_mul_f32 %1, %5, %5\n\tv_mul_f32 %3, %6, %6" : "+v"(p0), "+v"(t0), "+v"(p1), "+v"(t1), "=&v"(hp1) : "v"(acc[1][4]), "v"(acc[1][5]));
        SBAR();
        acc3 = MFMA32(cat8(wf4a, wf4b), xf[6], acc3); PIN(acc3); SBAR();
        wf3a = lds_tr(wa0 + 6144);
        wf3b = lds_tr(wa0 + 6656);
        asm volatile("v_fma_f32 %0, %0, %2, %3\n\tv_fma_f32 %1, %1, %2, %3\n\tv_mul_f32 %0, %0, %4\n\tv_mul_f32 %1, %1, %5" : "+v"(t0), "+v"(t1) : "v"(gk2), "v"(gk1), "v"(acc[1][4]), "v"(acc[1][5]));
        SBAR();
        acc3 = MFMA32(cat8(wf5a, wf5b), xf[7], acc3); PIN(acc3); SBAR();
        asm volatile("v_exp_f32 %0, %0\n\tv_exp_f32 %1, %1\n\tv_mul_f32 %2, %3, %4" : "+v"(t0), "+v"(t1), "=&v"(p0) : "v"(acc[1][4]), "v"(acc[2][4]));
        SBAR();
        waitv_bar<9>();
        acc4 = MFMA32(cat8(wf0a, wf0b), xf[4], acc4); PIN(acc4); SBAR();
        wf4a = lds_tr(wa1 + 24576);
        wf4b = lds_tr(wa1 + 25088);
        wf5a = lds_tr(wa1 + 26624);
        wf5b = lds_tr(wa1 + 27136);
        asm volatile("v_mul_f32 %0, %3, %4\n\tv_fma_f32 %1, %1, %5, %5\n\tv_fma_f32 %2, %2, %5, %5\n\tv_rcp_f32 %1, %1" : "=&v"(p1), "+v"(t0), "+v"(t1) : "v"(acc[1][5]), "v"(acc[2][5]), "v"(gvar));
        SBAR();
        acc4 = MFMA32(cat8(wf1a, wf1b), xf[5], acc4); PIN(acc4); SBAR();
        wf0a = lds_tr(wa1 + 28672);
        wf0b = lds_tr(wa1 + 29184);
        asm volatile("v_rcp_f32 %0, %0\n\tv_mul_f32 %1, %1, %4\n\tv_mul_f32 %2, %2, %0\n\tv_cvt_pk_bf16_f32 %3, %1, %2" : "+v"(t1), "+v"(p0), "+v"(p1), "=&v"(hp2) : "v"(t0));
        SBAR();
        waitv<0>();
        acc4 = MFMA32(cat8(wf2a, wf2b), xf[6], acc4); PIN(acc4); SBAR();
        wf1a = lds_tr(wa1 + 30720);
        wf1b = lds_tr(wa1 + 31232);
        asm volatile("v_mul_f32 %0, %2, %2\n\tv_mul_f32 %1, %3, %3\n\tv_fma_f32 %0, %0, %4, %5\n\tv_fma_f32 %1, %1, %4, %5\n\tv_mul_f32 %0, %0, %2" : "=&v"(t0), "=&v"(t1) : "v"(acc[1][6]), "v"(acc[1][7]), "v"(gk2), "v"(gk1));
        flag_store(ws, WS_FP + myflag, tagv);
        SBAR();
        acc4 = MFMA32(cat8(wf3a, wf3b), xf[7], acc4); PIN(acc4); SBAR();
        wf2a = lds_tr(wa0 + 16384);
        wf2b = lds_tr(wa0 + 16896);
        asm volatile("v_mul_f32 %0, %0, %2\n\tv_exp_f32 %1, %1\n\tv_exp_f32 %0, %0" : "+v"(t1), "+v"(t0) : "v"(acc[1][7]));
        SBAR();
        acc3 = MFMA32(cat8(wf4a, wf4b), xf[8], acc3); PIN(acc3); SBAR();
        wf3a = lds_tr(wa0 + 18432);
        wf3b = lds_tr(wa0 + 18944);
        asm volatile("v_mul_f32 %0, %4, %5\n\tv_mul_f32 %1, %6, %7\n\tv_fma_f32 %2, %2, %8, %8\n\tv_fma_f32 %3, %3, %8, %8" : "=&v"(p0), "=&v"(p1), "+v"(t0), "+v"(t1) : "v"(acc[1][6]), "v"(acc[2][6]), "v"(acc[1][7]), "v"(acc[2][7]), "v"(gvar));
        glds16i<49152>(sb4, vU, wave1k); vU += 229376u; PIN(vU);
        SBAR();
        acc3 = MFMA32(cat8(wf5a, wf5b), xf[9], acc3); PIN(acc3); SBAR();
        wf4a = lds_tr(wa0 + 20480);
        wf4b = lds_tr(wa0 + 20992);
        asm volatile("v_rcp_f32 %0, %0\n\tv_rcp_f32 %1, %1\n\tv_mul_f32 %2, %2, %0" : "+v"(t0), "+v"(t1), "+v"(p0) : );
        SBAR();
        acc3 = MFMA32(cat8(wf0a, wf0b), xf[10], acc3); PIN(acc3); SBAR();
        wf5a = lds_tr(wa0 + 22528);
        wf5b = lds_tr(wa0 + 23040);
        asm volatile("v_mul_f32 %0, %0, %2\n\tv_cvt_pk_bf16_f32 %1, %3, %0" : "+v"(p1), "=&v"(hp3) : "v"(t1), "v"(p0));
        gstore16_dev<0>(hdst, lane16, u32x4{hp0, hp1, hp2, hp3});
        asm volatile("v_mul_f32 %0, %2, %2\n\tv_mul_f32 %1, %3, %3" : "=&v"(t0), "=&v"(t1) : "v"(acc[1][8]), "v"(acc[1][9]));
        SBAR();
        acc3 = MFMA32(cat8(wf1a, wf1b), xf[11], acc3); PIN(acc3); SBAR();
        wf0a = lds_tr(wa0 + 24576);
        wf0b = lds_tr(wa0 + 25088);
        asm volatile("v_fma_f32 %0, %0, %2, %3\n\tv_fma_f32 %1, %1, %2, %3\n\tv_mul_f32 %0, %0, %4\n\tv_mul_f32 %1, %1, %5" : "+v"(t0), "+v"(t1) : "v"(gk2), "v"(gk1), "v"(acc[1][8]), "v"(acc[1][9]));
        SBAR();
        acc4 = MFMA32(cat8(wf2a, wf2b), xf[8], acc4); PIN(acc4); SBAR();
        wf1a = lds_tr(wa0 + 26624);
        wf1b = lds_tr(wa0 + 27136);
        asm volatile("v_exp_f32 %0, %0\n\tv_exp_f32 %1, %1\n\tv_mul_f32 %2, %3, %4" : "+v"(t0), "+v"(t1), "=&v"(p0) : "v"(acc[1][8]), "v"(acc[2][8]));
        SBAR();
        acc4 = MFMA32(cat8(wf3a, wf3b), xf[9], acc4); PIN(acc4); SBAR();
        wf2a = lds_tr(wa0 + 28672);
        wf2b = lds_tr(wa0 + 29184);
        asm volatile("v_mul_f32 %0, %3, %4\n\tv_fma_f32 %1, %1, %5, %5\n\tv_fma_f32 %2, %2, %5, %5\n\tv_rcp_f32 %1, %1" : "=&v"(p1), "+v"(t0), "+v"(t1) : "v"(acc[1][9]), "v"(acc[2][9]), "v"(gvar));
        SBAR();
        acc4 = MFMA32(cat8(wf4a, wf4b), xf[10], acc4); PIN(acc4); SBAR();
        wf3a = lds_tr(wa0 + 30720);
        wf3b = lds_tr(wa0 + 31232);
        asm volatile("v_rcp_f32 %0, %0\n\tv_mul_f32 %1, %1, %4\n\tv_mul_f32 %2, %2, %0\n\tv_cvt_pk_bf16_f32 %3, %1, %2" : "+v"(t1), "+v"(p0), "+v"(p1), "=&v"(hp0) : "v"(t0));
        glds16i<57344>(sb4, vU, wave1k);
        SBAR();
        acc4 = MFMA32(cat8(wf5a, wf5b), xf[11], acc4); PIN(acc4); SBAR();
        wf4a = lds_tr(wa1 + 32768);
        wf4b = lds_tr(wa1 + 33280);
        asm volatile("v_mul_f32 %0, %2, %2\n\tv_mul_f32 %1, %3, %3\n\tv_fma_f32 %0, %0, %4, %5\n\tv_fma_f32 %1, %1, %4, %5\n\tv_mul_f32 %0, %0, %2" : "=&v"(t0), "=&v"(t1) : "v"(acc[1][10]), "v"(acc[1][11]), "v"(gk2), "v"(gk1));
        SBAR();
        acc3 = MFMA32(cat8(wf0a, wf0b), xf[12], acc3); PIN(acc3); SBAR();
        wf5a = lds_tr(wa1 + 34816);
        wf5b = lds_tr(wa1 + 35328);
        asm volatile("v_mul_f32 %0, %0, %2\n\tv_exp_f32 %1, %1\n\tv_exp_f32 %0, %0" : "+v"(t1), "+v"(t0) : "v"(acc[1][11]));
        glds16i<G2_SA + 0>(spin(wd) + (UNIT(0) * 65536 + 0), vsrc2, wave4k);
        SBAR();
        acc3 = MFMA32(cat8(wf1a, wf1b), xf[13], acc3); PIN(acc3); SBAR();
        wf0a = lds_tr(wa1 + 36864);
        wf0b = lds_tr(wa1 + 37376);
        asm volatile("v_mul_f32 %0, %4, %5\n\tv_mul_f32 %1, %6, %7\n\tv_fma_f32 %2, %2, %8, %8\n\tv_fma_f32 %3, %3, %8, %8" : "=&v"(p0), "=&v"(p1), "+v"(t0), "+v"(t1) : "v"(acc[1][10]), "v"(acc[2][10]), "v"(acc[1][11]), "v"(acc[2][11]), "v"(gvar));
        SBAR();
        acc3 = MFMA32(cat8(wf2a, wf2b), xf[14], acc3); PIN(acc3); SBAR();
        wf1a = lds_tr(wa1 + 38912);
        wf1b = lds_tr(wa1 + 39424);
        asm volatile("v_rcp_f32 %0, %0\n\tv_rcp_f32 %1, %1\n\tv_mul_f32 %2, %2, %0" : "+v"(t0), "+v"(t1), "+v"(p0) : );
        glds16i<G2_SA + 1024>(spin(wd) + (UNIT(0) * 65536 + 8192), vsrc2, wave4k);
        SBAR();
        acc3 = MFMA32(cat8(wf3a, wf3b), xf[15], acc3); PIN(acc3); SBAR();
        asm volatile("v_mul_f32 %0, %0, %1\n\tv_cvt_pk_bf16_f32 %2, %4, %0\n\tv_mul_f32 %3, %5, %5\n\tv_mul_f32 %1, %6, %6\n\tv_fma_f32 %3, %3, %7, %8" : "+v"(p1), "+v"(t1), "=&v"(hp1), "=&v"(t0) : "v"(p0), "v"(acc[1][12]), "v"(acc[1][13]), "v"(gk2), "v"(gk1));
        SBAR();
        waitv_bar<11>();
                acc4 = MFMA32(cat8(wf4a, wf4b), xf[12], acc4); PIN(acc4); SBAR();
        wf2a = lds_tr(wa1 + 40960);
        wf2b = lds_tr(wa1 + 41472);
        wf3a = lds_tr(wa1 + 43008);
        wf3b = lds_tr(wa1 + 43520);
        asm volatile("v_fma_f32 %0, %0, %2, %3\n\tv_mul_f32 %1, %1, %4\n\tv_mul_f32 %0, %0, %5\n\tv_exp_f32 %1, %1" : "+v"(t1), "+v"(t0) : "v"(gk2), "v"(gk1), "v"(acc[1][12]), "v"(acc[1][13]));
        glds16i<G2_SA + 2048>(spin(wd) + (UNIT(0) * 65536 + 16384), vsrc2, wave4k);
        SBAR();
        acc4 = MFMA32(cat8(wf5a, wf5b), xf[13], acc4); PIN(acc4); SBAR();
        wf4a = lds_tr(wa1 + 45056);
        wf4b = lds_tr(wa1 + 45568);
        asm volatile("v_exp_f32 %0, %0\n\tv_mul_f32 %1, %4, %5\n\tv_mul_f32 %2, %6, %7\n\tv_fma_f32 %3, %3, %8, %8" : "+v"(t1), "=&v"(p0), "=&v"(p1), "+v"(t0) : "v"(acc[1][12]), "v"(acc[2][12]), "v"(acc[1][13]), "v"(acc[2][13]), "v"(gvar));
        T2LOAD;
        SBAR();
        acc4 = MFMA32(cat8(wf0a, wf0b), xf[14], acc4); PIN(acc4); SBAR();
        wf5a = lds_tr(wa1 + 47104);
        wf5b = lds_tr(wa1 + 47616);
        asm volatile("v_fma_f32 %0, %0, %2, %2\n\tv_rcp_f32 %1, %1\n\tv_rcp_f32 %0, %0" : "+v"(t1), "+v"(t0) : "v"(gvar));
        SBAR();
        acc4 = MFMA32(cat8(wf1a, wf1b), xf[15], acc4); PIN(acc4); SBAR();
        wf0a = lds_tr(wa1 + 49152);
        wf0b = lds_tr(wa1 + 49664);
        asm volatile("v_mul_f32 %0, %0, %1\n\tv_mul_f32 %2, %2, %3\n\tv_cvt_pk_bf16_f32 %4, %0, %2\n\tv_mul_f32 %1, %5, %5\n\tv_mul_f32 %3, %6, %6" : "+v"(p0), "+v"(t0), "+v"(p1), "+v"(t1), "=&v"(hp2) : "v"(acc[1][14]), "v"(acc[1][15]));
        glds16i<G2_SA + 3072>(spin(wd) + (UNIT(0) * 65536 + 24576), vsrc2, wave4k);
        SBAR();
        acc3 = MFMA32(cat8(wf2a, wf2b), xf[16], acc3); PIN(acc3); SBAR();
        wf1a = lds_tr(wa1 + 51200);
        wf1b = lds_tr(wa1 + 51712);
        asm volatile("v_fma_f32 %0, %0, %2, %3\n\tv_fma_f32 %1, %1, %2, %3\n\tv_mul_f32 %0, %0, %4\n\tv_mul_f32 %1, %1, %5" : "+v"(t0), "+v"(t1) : "v"(gk2), "v"(gk1), "v"(acc[1][14]), "v"(acc[1][15]));
        SBAR();
        acc3 = MFMA32(cat8(wf3a, wf3b), xf[17], acc3); PIN(acc3); SBAR();
        wf2a = lds_tr(wa1 + 53248);
        wf2b = lds_tr(wa1 + 53760);
        asm volatile("v_exp_f32 %0, %0\n\tv_exp_f32 %1, %1\n\tv_mul_f32 %2, %3, %4" : "+v"(t0), "+v"(t1), "=&v"(p0) : "v"(acc[1][14]), "v"(acc[2][14]));
        glds16i<G2_SB + 0>(spin(wd) + (UNIT(1) * 65536 + 0), vsrc2, wave4k);
        SBAR();
        acc3 = MFMA32(cat8(wf4a, wf4b), xf[18], acc3); PIN(acc3); SBAR();
        wf3a = lds_tr(wa1 + 55296);
        wf3b = lds_tr(wa1 + 55808);
        asm volatile("v_mul_f32 %0, %3, %4\n\tv_fma_f32 %1, %1, %5, %5\n\tv_fma_f32 %2, %2, %5, %5\n\tv_rcp_f32 %1, %1" : "=&v"(p1), "+v"(t0), "+v"(t1) : "v"(acc[1][15]), "v"(acc[2][15]), "v"(gvar));
        SBAR();
        acc3 = MFMA32(cat8(wf5a, wf5b), xf[19], acc3); PIN(acc3); SBAR();
        wf4a = lds_tr(wa1 + 57344);
        wf4b = lds_tr(wa1 + 57856);
        asm volatile("v_rcp_f32 %0, %0\n\tv_mul_f32 %1, %1, %4\n\tv_mul_f32 %2, %2, %0\n\tv_cvt_pk_bf16_f32 %3, %1, %2" : "+v"(t1), "+v"(p0), "+v"(p1), "=&v"(hp3) : "v"(t0));
        SBAR();
        acc3 = MFMA32(cat8(wf0a, wf0b), xf[20], acc3); PIN(acc3); SBAR();
        wf5a = lds_tr(wa1 + 59392);
        wf5b = lds_tr(wa1 + 59904);
        gstore16_dev<1024>(hdst, lane16, u32x4{hp0, hp1, hp2, hp3});
        glds16i<G2_SB + 1024>(spin(wd) + (UNIT(1) * 65536 + 8192), vsrc2, wave4k);
        SBAR();
        acc3 = MFMA32(cat8(wf1a, wf1b), xf[21], acc3); PIN(acc3); SBAR();
        wf0a = lds_tr(wa1 + 61440);
        wf0b = lds_tr(wa1 + 61952);
        SBAR();
    }
    SPIN2(ue, tagv);
    acc3 = MFMA32(cat8(wf2a, wf2b), xf[22], acc3); PIN(acc3); SBAR();
    wf1a = lds_tr(wa1 + 63488);
    wf1b = lds_tr(wa1 + 64000);
    PIN(lane16); const int ln = lane16 >> 4; unsigned vimg = AT_LDS_V + wave * 2048 + (ln >> 1) * 64 + (ln & 1) * 16, kaddr = AT_LDS_K + wave1k + lane16; PIN2(vimg, kaddr);
    SBAR();
    acc3 = MFMA32(cat8(wf3a, wf3b), xf[23], acc3); PIN(acc3); SBAR();
    wf2a = lds_tr(wa0 + 8192);
    wf2b = lds_tr(wa0 + 8704);
    unsigned hh512 = lane16 & 512u, rpC0 = G2_SC + wave4k + ((ln & 15) >> 2) * 128 + ((ln >> 3) & 1) * 64 + ((ln >> 4) & 1) * 32 + (ln & 3) * 8 + hh512; PIN2(hh512, rpC0);
    SBAR();
    acc3 = MFMA32(cat8(wf4a, wf4b), xf[24], acc3); PIN(acc3); SBAR();
    wf3a = lds_tr(wa0 + 10240);
    wf3b = lds_tr(wa0 + 10752);
    unsigned rpC1 = rpC0 ^ 64, rpA0 = rpC0 + (G2_SA - G2_SC), rpA1 = rpA0 ^ 64; PIN(rpC1); PIN2(rpA0, rpA1); const char* hdst2 = hdst + (4096u - 4096u * tsel); SPIN(hdst2);
    SBAR();
    acc3 = MFMA32(cat8(wf5a, wf5b), xf[25], acc3); PIN(acc3); SBAR();
    wf4a = lds_tr(wa0 + 12288);
    wf4b = lds_tr(wa0 + 12800);
    const char* a2o = ws + (WS_A2 + (unsigned)(tm2 * 2 * KSTEPS2 + 4 * wave) * 1024); const char* wo = wo_ + (unsigned)(wave * 65536 + tn2 * 128); SPIN2(a2o, wo);
    glds16i<G2_SB + 2048>(spin(wd) + (UNIT(1) * 65536 + 16384), vsrc2, wave4k);
    SBAR();
    acc3 = MFMA32(cat8(wf0a, wf0b), xf[26], acc3); PIN(acc3); SBAR();
    wf5a = lds_tr(wa0 + 14336);
    wf5b = lds_tr(wa0 + 14848);
    unsigned vs1 = vsrc2 + 7168, vs2 = vsrc2 + 14336, vs3 = vsrc2 + 21504; PIN2(vs1, vs2); PIN(vs3);
    SBAR();
    acc3 = MFMA32(cat8(wf1a, wf1b), xf[27], acc3); PIN(acc3); SBAR();
    unsigned vaddr = AT_LDS_V + wave * 2048 + (4 * (ln >> 5) + ((ln & 15) >> 2)) * 64 + ((ln >> 4) & 1) * 32 + (ln & 3) * 8; PIN(vaddr);
    SBAR();
    waitT<4, 1>(tsel);
    waitv_bar<5>();
        acc3 = MFMA32(cat8(wf2a, wf2b), xf[28], acc3); PIN(acc3); SBAR();
    wf0a = lds_tr(wa0 + 32768);
    wf0b = lds_tr(wa0 + 33280);
    wf1a = lds_tr(wa0 + 34816);
    wf1b = lds_tr(wa0 + 35328);
    T2READ;
    int pb = ((tm2 >> 1) * 16 + (wave >> 1) * 4) * 8 + (wave & 1) * 4 + (tm2 & 1) * 2 + (ln & 1), pj = (ln >> 1) & 3; PIN2(pb, pj);
    glds16i<G2_SB + 3072>(spin(wd) + (UNIT(1) * 65536 + 24576), vsrc2, wave4k);
    SBAR();
    acc3 = MFMA32(cat8(wf3a, wf3b), xf[29], acc3); PIN(acc3); SBAR();
    wf2a = lds_tr(wa0 + 36864);
    wf2b = lds_tr(wa0 + 37376);
    unsigned fe_off = WS_F2 + (pb + (pj < 3 ? pj : 0) * 8) * 128, fl_off = WS_F3 + (pb + pj * 8) * 128; PIN2(fe_off, fl_off);
    SBAR();
    acc3 = MFMA32(cat8(wf4a, wf4b), xf[30], acc3); PIN(acc3); SBAR();
    wf3a = lds_tr(wa0 + 38912);
    wf3b = lds_tr(wa0 + 39424);
    unsigned fo_ = WS_FP + ((((wave >> 2) * 8 + (tm2 >> 2)) * 16 + (wave & 3) * 4 + pj) * 8 + 2 * (tm2 & 3) + (ln & 1)) * 128; PIN(fo_);
    BAD2;
    gload8_dev(pq, ws, fq_off);
    SBAR();
    acc3 = MFMA32(cat8(wf5a, wf5b), xf[31], acc3); PIN(acc3); SBAR();
    wf4a = lds_tr(wa0 + 40960);
    wf4b = lds_tr(wa0 + 41472);
    fl_off = (ln >> 3) == 1 ? fo_ : fl_off; fo_ = WS_FP + (wave * 32 + ln - 32) * 1024; PIN2(fl_off, fo_); fl_off = b == 0 && ln >= 32 ? fo_ : fl_off; PIN(fl_off);
    CHECK2;
    ca0 = CDUP(ca0); PIN(ca0);
    SBAR();
    acc3 = MFMA32(ca0, MEAN, acc3); PIN(acc3); SBAR();
    acc4 = MFMA32(cat8(wf0a, wf0b), xf[16], acc4); PIN(acc4); SBAR();
    wf5a = lds_tr(wa0 + 43008);
    wf5b = lds_tr(wa0 + 43520);
    const char* a2h = ws + (WS_A2 + (unsigned)(tm2 * 2 * KSTEPS2 + 32 + 28 * (wave >> 1) + 2 * (wave & 1)) * 1024); unsigned vb2 = lane16 + KSTEPS2 * 1024; SPIN(a2h); PIN(vb2);
    ca1 = CDUP(ca1); PIN(ca1);
    SBAR();
    acc4 = MFMA32(ca1, MEAN, acc4); PIN(acc4); SBAR();
    acc4 = MFMA32(cat8(wf1a, wf1b), xf[17], acc4); PIN(acc4); SBAR();
    wf0a = lds_tr(wa0 + 45056);
    wf0b = lds_tr(wa0 + 45568);
    unsigned one_ = (ln & 1) ? 0u : 0x3f80u, off = (unsigned)(((tm2 * 64 + wave * 8 + (ln >> 3)) * DM + tn2 * 64 + (ln & 7) * 8) * 2); PIN2(one_, off);
    SBAR();
    acc4 = MFMA32(cat8(wf2a, wf2b), xf[18], acc4); PIN(acc4); SBAR();
    wf1a = lds_tr(wa0 + 47104);
    wf1b = lds_tr(wa0 + 47616);
    unsigned ra = (unsigned)(((wave >> 2) * 2 + ((ln >> 2) & 1)) * 32768 + (ln & 3) * 1024 + ((((wave & 3) * 8 + (ln >> 3)) * 16) ^ ((ln & 3) * 64))); PIN(ra);
    SBAR();
    acc4 = MFMA32(cat8(wf3a, wf3b), xf[19], acc4); PIN(acc4); SBAR();
    wf2a = lds_tr(wa0 + 49152);
    wf2b = lds_tr(wa0 + 49664);
    asm volatile("v_mul_f32 %0, %2, %2\n\tv_mul_f32 %1, %3, %3\n\tv_fma_f32 %0, %0, %4, %5\n\tv_fma_f32 %1, %1, %4, %5\n\tv_mul_f32 %0, %0, %2\n\tv_mul_f32 %1, %1, %3" : "=&v"(t0), "=&v"(t1) : "v"(acc3[8]), "v"(acc3[9]), "v"(gk2), "v"(gk1));
    SBAR();
    acc4 = MFMA32(cat8(wf4a, wf4b), xf[20], acc4); PIN(acc4); SBAR();
    wf3a = lds_tr(wa0 + 51200);
    wf3b = lds_tr(wa0 + 51712);
    asm volatile("v_exp_f32 %0, %0\n\tv_exp_f32 %1, %1\n\tv_fma_f32 %0, %0, %2, %2\n\tv_fma_f32 %1, %1, %2, %2" : "+v"(t0), "+v"(t1) : "v"(gvar));
    SBAR();
    acc4 = MFMA32(cat8(wf5a, wf5b), xf[21], acc4); PIN(acc4); SBAR();
    wf4a = lds_tr(wa0 + 53248);
    wf4b = lds_tr(wa0 + 53760);
    asm volatile("v_rcp_f32 %0, %0\n\tv_rcp_f32 %1, %1\n\tv_mul_f32 %2, %4, %0\n\tv_mul_f32 %3, %5, %1" : "+v"(t0), "+v"(t1), "=&v"(sg[8]), "=&v"(sg[9]) : "v"(acc3[8]), "v"(acc3[9]));
    SBAR();
    acc4 = MFMA32(cat8(wf0a, wf0b), xf[22], acc4); PIN(acc4); SBAR();
    wf5a = lds_tr(wa0 + 55296);
    wf5b = lds_tr(wa0 + 55808);
    asm volatile("v_mul_f32 %0, %2, %2\n\tv_mul_f32 %1, %3, %3\n\tv_fma_f32 %0, %0, %4, %5\n\tv_fma_f32 %1, %1, %4, %5\n\tv_mul_f32 %0, %0, %2\n\tv_mul_f32 %1, %1, %3" : "=&v"(t0), "=&v"(t1) : "v"(acc3[10]), "v"(acc3[11]), "v"(gk2), "v"(gk1));
    SBAR();
    wait1<0>(pq); poll_flags(ws, fq_off, pq, tagv);
    acc4 = MFMA32(cat8(wf1a, wf1b), xf[23], acc4); PIN(acc4); SBAR();
    wf0a = lds_tr(wa0 + 57344);
    wf0b = lds_tr(wa0 + 57856);
    asm volatile("v_exp_f32 %0, %0\n\tv_exp_f32 %1, %1\n\tv_fma_f32 %0, %0, %2, %2\n\tv_fma_f32 %1, %1, %2, %2" : "+v"(t0), "+v"(t1) : "v"(gvar));
    KVLOAD0;
    SBAR();
    acc4 = MFMA32(cat8(wf2a, wf2b), xf[24], acc4); PIN(acc4); SBAR();
    wf1a = lds_tr(wa0 + 59392);
    wf1b = lds_tr(wa0 + 59904);
    asm volatile("v_rcp_f32 %0, %0\n\tv_rcp_f32 %1, %1\n\tv_mul_f32 %2, %4, %0\n\tv_mul_f32 %3, %5, %1" : "+v"(t0), "+v"(t1), "=&v"(sg[10]), "=&v"(sg[11]) : "v"(acc3[10]), "v"(acc3[11]));
    KVLOAD1;
    SBAR();
    acc4 = MFMA32(cat8(wf3a, wf3b), xf[25], acc4); PIN(acc4); SBAR();
    wf2a = lds_tr(wa0 + 61440);
    wf2b = lds_tr(wa0 + 61952);
    asm volatile("v_mul_f32 %0, %2, %2\n\tv_mul_f32 %1, %3, %3\n\tv_fma_f32 %0, %0, %4, %5\n\tv_fma_f32 %1, %1, %4, %5\n\tv_mul_f32 %0, %0, %2\n\tv_mul_f32 %1, %1, %3" : "=&v"(t0), "=&v"(t1) : "v"(acc3[12]), "v"(acc3[13]), "v"(gk2), "v"(gk1));
    KVLOAD2;
    SBAR();
    acc4 = MFMA32(cat8(wf4a, wf4b), xf[26], acc4); PIN(acc4); SBAR();
    wf3a = lds_tr(wa0 + 63488);
    wf3b = lds_tr(wa0 + 64000);
    asm volatile("v_exp_f32 %0, %0\n\tv_exp_f32 %1, %1\n\tv_fma_f32 %0, %0, %2, %2\n\tv_fma_f32 %1, %1, %2, %2" : "+v"(t0), "+v"(t1) : "v"(gvar));
    KVLOAD3;
    SBAR();
    acc4 = MFMA32(cat8(wf5a, wf5b), xf[27], acc4); PIN(acc4); SBAR();
    asm volatile("v_rcp_f32 %0, %0\n\tv_rcp_f32 %1, %1\n\tv_mul_f32 %2, %4, %0\n\tv_mul_f32 %3, %5, %1" : "+v"(t0), "+v"(t1), "=&v"(sg[12]), "=&v"(sg[13]) : "v"(acc3[12]), "v"(acc3[13]));
    KVLOAD4;
    SBAR();
    acc4 = MFMA32(cat8(wf0a, wf0b), xf[28], acc4); PIN(acc4); SBAR();
    asm volatile("v_mul_f32 %0, %2, %2\n\tv_mul_f32 %1, %3, %3\n\tv_fma_f32 %0, %0, %4, %5\n\tv_fma_f32 %1, %1, %4, %5\n\tv_mul_f32 %0, %0, %2\n\tv_mul_f32 %1, %1, %3" : "=&v"(t0), "=&v"(t1) : "v"(acc3[14]), "v"(acc3[15]), "v"(gk2), "v"(gk1));
    SBAR();
    acc4 = MFMA32(cat8(wf1a, wf1b), xf[29], acc4); PIN(acc4); SBAR();
    asm volatile("v_exp_f32 %0, %0\n\tv_exp_f32 %1, %1\n\tv_fma_f32 %0, %0, %2, %2\n\tv_fma_f32 %1, %1, %2, %2" : "+v"(t0), "+v"(t1) : "v"(gvar));
    SBAR();
    acc4 = MFMA32(cat8(wf2a, wf2b), xf[30], acc4); PIN(acc4); SBAR();
    asm volatile("v_rcp_f32 %0, %0\n\tv_rcp_f32 %1, %1\n\tv_mul_f32 %2, %4, %0\n\tv_mul_f32 %3, %5, %1" : "+v"(t0), "+v"(t1), "=&v"(sg[14]), "=&v"(sg[15]) : "v"(acc3[14]), "v"(acc3[15]));
    SBAR();
    acc4 = MFMA32(cat8(wf3a, wf3b), xf[31], acc4); PIN(acc4); SBAR();
    SBAR();


    {
    bf16x8 ta[9][4];
    {
        u32x2 fl, fv;
        unsigned hq[8];
        auto gelu = [&](int i) {
            float h[2];
#pragma unroll
            for (int z = 0; z < 2; ++z) {
                const float ga = acc3[2 * i + z], ua = acc4[2 * i + z];
                const float e = __builtin_amdgcn_exp2f(ga * __builtin_fmaf(ga * ga, gk2, gk1));
                h[z] = ga * __builtin_amdgcn_rcpf(__builtin_fmaf(e, gvar, gvar)) * ua;
            }
            hq[i] = cvt_pk_bf16(h[0], h[1]);
        };
        const unsigned one0_ = tg > 0 ? one_ : 0u;
#define ONES0 lds_st128(vimg + 32, u32x4{one0_, 0u, one0_, 0u})
#define ONES1 lds_st128(vimg + 16384 + 32, u32x4{one_, 0u, one_, 0u})
#define GELUF(i) PIN(gvar); gelu(i); PIN(hq[i])
#define GELUB(i) hq[i] = cvt_pk_bf16(sg[2 * (i)] * acc4[2 * (i)], sg[2 * (i) + 1] * acc4[2 * (i) + 1]); PIN(hq[i])
#define HSTORE(z) gstore16_dev<(z) * 1024>(hdst2, lane16, u32x4{hq[4 * (z)], hq[4 * (z) + 1], hq[4 * (z) + 2], hq[4 * (z) + 3]})
#define KVSTAGE0 asm volatile("" :: "v"(tsink)); lds_st128(kaddr, __builtin_bit_cast(u32x4, kv0))
#define KVSTAGE1 lds_st128(kaddr + 8192, __builtin_bit_cast(u32x4, kv1))
#define KVSTAGE2 { u32x4 v0 = __builtin_bit_cast(u32x4, kv2); if (tg == 0) v0 = u32x4{0u, 0u, 0u, 0u}; lds_st128(vimg, v0); }
#define KVSTAGE3 lds_st128(vimg + 16384, __builtin_bit_cast(u32x4, kv3))
        bf16x8 kf[9], vf[2], vg[2], vc, vn;
        f32x16 sm, sa, sb, negm, o;
        float m, m2;
        u32x4 pd[2], pf[2];
#define KREAD kf[0] = lds_b128(kaddr); kf[8] = lds_b128(kaddr + 8192); kf[1] = lds_b128(kaddr + 1024); vf[0] = VRD(8, 0); vf[1] = VRD(0, 0)
#define MERGE { const f32x16 sf = MFMA32(kf[0], qf, zero16); const f32x16 sd = MFMA32(kf[8], qf, zero16); \
            _Pragma("unroll") for (int r = 0; r < 16; ++r) sm[r] = vis(r) ? sd[r] : sf[r]; }
#define MAX16(v) _Pragma("unroll") for (int r = 0; r < 16; r += 4) { m = max3(m, v[r], v[r + 1]); m2 = max3(m2, v[r + 2], v[r + 3]); }
#define MAXEND { m = fmaxf(m, m2); const auto sw = __builtin_amdgcn_permlane32_swap(__float_as_uint(m), __float_as_uint(m), false, false); \
            m = fmaxf(__uint_as_float(sw[0]), __uint_as_float(sw[1])); }
#define MAXB { MERGE; m = max3(sm[0], sm[1], sm[2]); m2 = max3(sm[3], sm[4], sm[5]); \
            _Pragma("unroll") for (int r = 6; r < 14; r += 4) { m = max3(m, sm[r], sm[r + 1]); m2 = max3(m2, sm[r + 2], sm[r + 3]); } \
            m = max3(m, sm[14], sm[15]); MAXEND; _Pragma("unroll") for (int r = 0; r < 16; ++r) negm[r] = -m; PIN2(m, negm); PIN(sm); }
#define VRD(j, h) cat8(lds_tr(vaddr + (j) * 2048 + (h) * 1024), lds_tr(vaddr + (j) * 2048 + (h) * 1024 + 512))
#define EX(v) __builtin_amdgcn_exp2f(v)
#define PVB0(h) _Pragma("unroll") for (int r = 8 * (h); r < 8 * (h) + 8; r += 2) { \
                const unsigned pk = cvt_pk_bf16(EX(sm[r] - m), EX(sm[r + 1] - m)); \
                const unsigned lo = pk & 0xffffu, t0 = vis(r) ? lo : 0u, dd = vis(r + 1) ? pk : t0; \
                pd[h][(r >> 1) & 3] = dd; pf[h][(r >> 1) & 3] = pk ^ dd; } \
            o = MFMA32(vf[0], __builtin_bit_cast(bf16x8, pd[h]), (h) == 0 ? zero16 : o); o = MFMA32(vf[1], __builtin_bit_cast(bf16x8, pf[h]), o);
#define PVB(h) { if ((h) == 1) { sb = MFMA32(kf[1], qf, negm); PIN(sb); vn = VRD(1, 0); kf[2] = lds_b128(kaddr + 2048); SBAR(); } else { vg[0] = VRD(8, 1); vg[1] = VRD(0, 1); SBAR(); } \
            PVB0(h) PIN(o); vf[0] = vg[0]; vf[1] = vg[1]; }
#define PVS0(j, h) { f32x16& cur = (j) & 1 ? sb : sa; f32x16& nxt = (j) & 1 ? sa : sb; vc = vn; \
            if ((h) == 1 && (j) < 7) { nxt = MFMA32(kf[(j) < 7 ? (j) + 1 : 0], qf, negm); PIN(nxt); } \
            if ((j) < 7 || (h) == 0) vn = VRD((j) + (h), 1 - (h)); \
            if ((h) == 1 && (j) < 6) kf[(j) + 2] = lds_b128(kaddr + ((j) + 2) * 1024); \
            SBAR(); \
            _Pragma("unroll") for (int r = 0; r < 2; ++r) pd[h][r] = cvt_pk_bf16(EX(cur[8 * (h) + 2 * r]), EX(cur[8 * (h) + 2 * r + 1])); PIN(pd[h]); }
#define PVS1(j, h) { f32x16& cur = (j) & 1 ? sb : sa; \
            _Pragma("unroll") for (int r = 2; r < 4; ++r) pd[h][r] = cvt_pk_bf16(EX(cur[8 * (h) + 2 * r]), EX(cur[8 * (h) + 2 * r + 1])); \
            o = MFMA32(vc, __builtin_bit_cast(bf16x8, pd[h]), o); PIN(o); }
#define REDO { float c0 = o[8], c1 = 0.f; \
            _Pragma("unroll") for (int r = 0; r < 8; r += 2) { c0 = __builtin_fmaf(o[r], 0.f, c0); c1 = __builtin_fmaf(o[r + 1], 0.f, c1); }        \
            if (__builtin_expect(__any(!(c0 + c1 <= 1e30f)), 0)) { \
                asm volatile("s_waitcnt vmcnt(0)" ::: "memory"); SBAR(); \
                m2 = m; \
                _Pragma("clang loop unroll(disable)") for (int j = 1; j < 8; ++j) { sa = MFMA32(lds_b128(kaddr + j * 1024), qf, zero16); MAX16(sa) } \
                MAXEND; kf[0] = lds_b128(kaddr); kf[8] = lds_b128(kaddr + 8192); MERGE; \
                vf[0] = VRD(8, 0); vf[1] = VRD(0, 0); PVB0(0) vf[0] = VRD(8, 1); vf[1] = VRD(0, 1); PVB0(1) \
                _Pragma("clang loop unroll(disable)") for (int j = 1; j < 8; ++j) { sa = MFMA32(lds_b128(kaddr + j * 1024), qf, zero16); \
                    _Pragma("unroll") for (int h = 0; h < 2; ++h) { \
                        _Pragma("unroll") for (int r = 0; r < 4; ++r) pd[h][r] = cvt_pk_bf16(EX(sa[8 * h + 2 * r] - m), EX(sa[8 * h + 2 * r + 1] - m)); \
                        o = MFMA32(VRD(j, h), __builtin_bit_cast(bf16x8, pd[h]), o); } } \
            } PIN(o); }
#define OSTORE { const float rl = __builtin_amdgcn_rcpf(o[8]); \
            gstore16_dev(odst, lane16, u32x4{min(cvt_pk_bf16(o[0] * rl, o[1] * rl), ~1u), min(cvt_pk_bf16(o[2] * rl, o[3] * rl), ~1u), \
                                             min(cvt_pk_bf16(o[4] * rl, o[5] * rl), ~1u), min(cvt_pk_bf16(o[6] * rl, o[7] * rl), ~1u)}); }
        f32x16 ac[2][2];
        auto mm = [&](int P, int e, int c, int tt) {
            ac[c][tt] = MFMA32(cat8(w2h[P][2 * e + c][0], w2h[P][2 * e + c][1]), ta[P][2 * e + tt], (P == 0 && e == 0) ? zero16 : ac[c][tt]);
            PIN(ac[c][tt]); SBAR();
        };
#define WT(P, N) wait4<N>(ta[P][0], ta[P][1], ta[P][2], ta[P][3])
        u32x4 xv;
        unsigned pw[4];
#define PWINIT _Pragma("unroll") for (int g4 = 0; g4 < 4; ++g4) { pw[g4] = wave4k + g4 * 1024 + (lane16 ^ (g4 * 64)); PIN(pw[g4]); }
#define PWNEXT _Pragma("unroll") for (int g4 = 0; g4 < 4; ++g4) { pw[g4] += 65536; PIN(pw[g4]); }
        auto wr = [&](int tt, int k) {
            const int ct = k >> 2, g4 = k & 3;
            const f32x4 v = {ac[ct][tt][4 * g4], ac[ct][tt][4 * g4 + 1], ac[ct][tt][4 * g4 + 2], ac[ct][tt][4 * g4 + 3]};
            *(LAS f32x4*)(uintptr_t)(pw[g4] + ct * 32768) = v;
        };
#define OCHECK(t) _Pragma("clang loop unroll(disable)") for (unsigned spins = 0; spins < SPIN_CAP; ++spins) { \
            if (!__any(umax_frag(umax_frag(umax_frag(umax_frag(0u, ta[7][t]), ta[7][2 + t]), ta[8][t]), ta[8][2 + t]) == ~0u)) break; \
            __builtin_amdgcn_s_sleep(2); ABASE(7); issueA(7, t); issueA(7, 2 + t); ABASE(8); issueA(8, t); issueA(8, 2 + t); \
            wait4<0>(ta[7][t], ta[7][2 + t], ta[8][t], ta[8][2 + t]); }
        SBAR();
        waitv<5>();
        flag_store(ws, WS_F2 + myflag, tagv);
        SBAR();
        ONES0; GELUF(0);GELUF(1);
        SBAR();
        ONES1; GELUF(2);GELUF(3);
        HSTORE(0);
                SBAR();
        wait4<2>(kv0, kv1, kv2, kv3); wait1<2>(qf);
        KVSTAGE0; GELUB(4);
        SBAR(); KVSTAGE1; GELUB(5);
        SBAR();
        waitT<12, 1>(tsel);
        waitv<13>();
        rdw(0, 0); rdw(0, 1); rdw(0, 2); rdw(0, 3); rdw(0, 4); rdw(0, 5); rdw(0, 6); rdw(0, 7);
        SBAR();
        KVSTAGE2; GELUB(6);
        SBAR(); KVSTAGE3; GELUB(7);
        HSTORE(1);
        SBAR();
        LGKM_BAR();         KREAD;
        SBAR();
        __builtin_amdgcn_s_setprio(3);
        SETM0(2); WBASE(2); issueW(0);
        issueW(1);
        SBAR();
        MAXB;
                SBAR();
        issueW(2);
        issueW(3);
        waitv<13>();
        rdw(1, 0); rdw(1, 1); rdw(1, 2); rdw(1, 3); rdw(1, 4); rdw(1, 5); rdw(1, 6); rdw(1, 7);
        SBAR();
        PVB(0);
        SBAR();
        SETM0(3); WBASE(3); issueW(0);
        issueW(1);
        SBAR();
        PVB(1);
        SBAR();
        issueW(2);
        issueW(3);
                SBAR();
        PVS0(1, 0);
        SBAR();
        PVS1(1, 0);
        SBAR();
        waitv<8>();
        flag_store(ws, WS_F3 + myflag, tagv);
        LGKM0();
        SETM0(4); WBASE(4); issueW(0);
        issueW(1);
        SBAR();
        PVS0(1, 1);
        SBAR();
        PVS1(1, 1);
        SBAR();
        issueW(2);
        issueW(3);
        gload8_dev(fv, ws, fe_off);
        SBAR();
        PVS0(2, 0);
        SBAR();
        PVS1(2, 0);
        SBAR();
        waitv<10>();
        rdw(2, 0); rdw(2, 1); rdw(2, 2); rdw(2, 3); rdw(2, 4); rdw(2, 5); rdw(2, 6); rdw(2, 7);
        __builtin_amdgcn_s_setprio(2);
        SBAR();
        PVS0(2, 1);
        SBAR();
        PVS1(2, 1);
        SBAR();
        LGKM0();
        SETM0(5); WBASE(5); issueW(0);
        issueW(1);
        SBAR();
        PVS0(3, 0);
        SBAR();
        PVS1(3, 0);
        SBAR();
        issueW(2);
        issueW(3);
        SBAR();
        PVS0(3, 1);
        SBAR();
        PVS1(3, 1);
        SBAR();
        wait1<4>(fv); poll_flags(ws, fe_off, fv, tagv);
        ABASE(0); issueA(0, 0);
        issueA(0, 1);
        SBAR();
        PVS0(4, 0);
        SBAR();
        PVS1(4, 0);
        SBAR();
        issueA(0, 2);
        issueA(0, 3);
        SBAR();
        PVS0(4, 1);
        SBAR();
        PVS1(4, 1);
        SBAR();
        ABASE(1); issueA(1, 0);
        issueA(1, 1);
        SBAR();
        PVS0(5, 0);
        SBAR();
        PVS1(5, 0);
        SBAR();
        __builtin_amdgcn_s_setprio(1);
        issueA(1, 2);
        issueA(1, 3);
        SBAR();
        PVS0(5, 1);
        SBAR();
        PVS1(5, 1);
        SBAR();
        ABASE(2); issueA(2, 0);
        issueA(2, 1);
        SBAR();
        PVS0(6, 0);
        SBAR();
        PVS1(6, 0);
        SBAR();
        issueA(2, 2);
        issueA(2, 3);
        SBAR();
        PVS0(6, 1);
        SBAR();
        PVS1(6, 1);
        SBAR();
        PVS0(7, 0);
        SBAR();
        PVS1(7, 0);
        SBAR();
        gload8_dev(fl, ws, fl_off);
        SBAR();
        PVS0(7, 1);
        SBAR();
        PVS1(7, 1);
        SBAR();
        REDO;
        OSTORE;
                SBAR();
        __builtin_amdgcn_s_setprio(0);
                wait1<1>(fl); poll_flags(ws, fl_off, fl, tagv);
        ABASE(3); issueA(3, 0);
        issueA(3, 1);
        issueA(3, 2);
        SBAR();
        WT(0, 13);
        mm(0, 0, 0, 0);
        SBAR();
        mm(0, 0, 0, 1);
        SBAR();
        mm(0, 0, 1, 0);
        SBAR();
        issueA(3, 3);
        SBAR();
        mm(0, 0, 1, 1);
        SBAR();
        mm(0, 1, 0, 0);
        SBAR();
        ABASE(4); issueA(4, 0);
        SBAR();
        mm(0, 1, 0, 1);
        SBAR();
        mm(0, 1, 1, 0);
        SBAR();
        issueA(4, 1);
        SBAR();
        mm(0, 1, 1, 1);
                SBAR();
        WT(1, 12);
        waitv<30>();
        asm volatile("s_barrier" ::: "memory");
        mm(1, 0, 0, 0); rdw(3, 0); rdw(3, 1);
        SBAR();
        issueA(4, 2);
        SBAR();
        mm(1, 0, 0, 1); rdw(3, 2); rdw(3, 3);
        SBAR();
        mm(1, 0, 1, 0); rdw(3, 4); rdw(3, 5);
        SBAR();
        issueA(4, 3);
        SBAR();
        mm(1, 0, 1, 1); rdw(3, 6); rdw(3, 7);
        SBAR();
        mm(1, 1, 0, 0);
        SBAR();
        ABASE(5); issueA(5, 0);
        SBAR();
        mm(1, 1, 0, 1);
        SBAR();
        mm(1, 1, 1, 0);
        SBAR();
        LGKM0();
        SETM0(6); WBASE(6); issueW(0);
        SBAR();
        mm(1, 1, 1, 1);
        SBAR();
        WT(2, 12);
        waitv<29>();
        mm(2, 0, 0, 0); rdw(4, 0); rdw(4, 1);
        SBAR();
        issueW(1);
        SBAR();
        mm(2, 0, 0, 1); rdw(4, 2); rdw(4, 3);
        SBAR();
        mm(2, 0, 1, 0); rdw(4, 4); rdw(4, 5);
        SBAR();
        issueW(2);
        SBAR();
        mm(2, 0, 1, 1); rdw(4, 6); rdw(4, 7);
        SBAR();
        mm(2, 1, 0, 0);
        SBAR();
        issueW(3);
        SBAR();
        mm(2, 1, 0, 1);
        SBAR();
        mm(2, 1, 1, 0);
        SBAR();
        issueA(5, 1);
        SBAR();
        mm(2, 1, 1, 1);
        SBAR();
                issueA(5, 2);
        issueA(5, 3);
        SBAR();
        WT(3, 12);
        mm(3, 0, 0, 0);
        SBAR();
        ABASE(6); issueA(6, 0);
        waitv<31>();
        rdw(5, 0); rdw(5, 1); rdw(5, 2); rdw(5, 3); rdw(5, 4); rdw(5, 5); rdw(5, 6); rdw(5, 7);
        SBAR();
        mm(3, 0, 0, 1);
        SBAR();
        mm(3, 0, 1, 0);
        SBAR();
        issueA(6, 1);
        SBAR();
        mm(3, 0, 1, 1);
        SBAR();
        mm(3, 1, 0, 0);
        SBAR();
        issueA(6, 2);
        SBAR();
        mm(3, 1, 0, 1);
        SBAR();
        mm(3, 1, 1, 0);
        SBAR();
        issueA(6, 3);
        SBAR();
        mm(3, 1, 1, 1);
        SBAR();
        asm volatile("s_waitcnt lgkmcnt(8)" ::: "memory");
        SETM0(7); WBASE(7); issueW(0);
        issueW(1);
        SBAR();
        WT(4, 14);
        mm(4, 0, 0, 0);
        SBAR();
        issueW(2);
        SBAR();
        mm(4, 0, 0, 1);
        SBAR();
        mm(4, 0, 1, 0);
        SBAR();
        issueW(3);
        SBAR();
        mm(4, 0, 1, 1);
        SBAR();
        mm(4, 1, 0, 0);
        SBAR();
        LGKM0();
        SETM0(8); WBASE(8); issueW(0);
        SBAR();
        mm(4, 1, 0, 1);
        SBAR();
        mm(4, 1, 1, 0);
        SBAR();
        issueW(1);
        waitv<13>();
        rdw(6, 0); rdw(6, 1); rdw(6, 2); rdw(6, 3); rdw(6, 4); rdw(6, 5); rdw(6, 6); rdw(6, 7);
        SBAR();
        mm(4, 1, 1, 1);
        SBAR();
                issueW(2);
        issueW(3);
        gload16<0>(xv, xp, off);
        ABASE(7); issueA(7, 0);
        SBAR();
        WT(5, 14);
        mm(5, 0, 0, 0);
        SBAR();
        issueA(7, 2);
        SBAR();
        mm(5, 0, 0, 1);
        SBAR();
        mm(5, 0, 1, 0);
        SBAR();
        ABASE(8); issueA(8, 0);
        SBAR();
        mm(5, 0, 1, 1);
        SBAR();
        mm(5, 1, 0, 0);
        SBAR();
        issueA(8, 2);
        SBAR();
        mm(5, 1, 0, 1);
        SBAR();
        mm(5, 1, 1, 0);
        SBAR();
        ABASE(7); issueA(7, 1);
        waitv<10>();
        rdw(7, 0); rdw(7, 1); rdw(7, 2); rdw(7, 3); rdw(7, 4); rdw(7, 5); rdw(7, 6); rdw(7, 7);
        SBAR();
        mm(5, 1, 1, 1);
        SBAR();
        issueA(7, 3);
        ABASE(8); issueA(8, 1);
        issueA(8, 3);
        SBAR();
        WT(6, 17);
        mm(6, 0, 0, 0);
        SBAR();
        mm(6, 0, 0, 1);
        SBAR();
        mm(6, 0, 1, 0);
        SBAR();
        mm(6, 0, 1, 1);
        SBAR();
        mm(6, 1, 0, 0);
        SBAR();
        mm(6, 1, 0, 1);
        SBAR();
        mm(6, 1, 1, 0);
        SBAR();
        waitv<9>();
        rdw(8, 0); rdw(8, 1); rdw(8, 2); rdw(8, 3); rdw(8, 4); rdw(8, 5); rdw(8, 6); rdw(8, 7);
        SBAR();
        mm(6, 1, 1, 1);
        SBAR();
        PWINIT;
        wait4<4>(ta[7][0], ta[7][2], ta[8][0], ta[8][2]); OCHECK(0);
        mm(7, 0, 0, 0); mm(7, 1, 0, 0); mm(8, 0, 0, 0); mm(8, 1, 0, 0); LGKM0();
        mm(7, 0, 1, 0); mm(7, 1, 1, 0); wr(0, 0); SBAR(); mm(8, 0, 1, 0); wr(0, 1); SBAR(); mm(8, 1, 1, 0); wr(0, 2); wr(0, 3); SBAR();
                wait4<0>(ta[7][1], ta[7][3], ta[8][1], ta[8][3]); PIN(xv); OCHECK(1);
        mm(7, 0, 0, 1); wr(0, 4); SBAR(); mm(7, 1, 0, 1); wr(0, 5); SBAR(); mm(8, 0, 0, 1); wr(0, 6); SBAR(); mm(8, 1, 0, 1); wr(0, 7); SBAR(); PWNEXT;
        mm(7, 0, 1, 1); mm(7, 1, 1, 1); wr(1, 0); SBAR(); mm(8, 0, 1, 1); wr(1, 1); SBAR(); mm(8, 1, 1, 1); wr(1, 2); wr(1, 3); SBAR();
        wr(1, 4); wr(1, 5); wr(1, 6); wr(1, 7);
        f32x4 s0 = {__uint_as_float(xv[0] << 16), __uint_as_float(xv[0] & 0xffff0000u), __uint_as_float(xv[1] << 16), __uint_as_float(xv[1] & 0xffff0000u)};
        f32x4 s1 = {__uint_as_float(xv[2] << 16), __uint_as_float(xv[2] & 0xffff0000u), __uint_as_float(xv[3] << 16), __uint_as_float(xv[3] & 0xffff0000u)};
        PIN2(s0, s1);
                LGKM_BAR();
        #pragma unroll
        for (int w = 0; w < 8; ++w) {
            s0 += *(const LAS f32x4*)(uintptr_t)(ra + w * 4096);
            s1 += *(const LAS f32x4*)(uintptr_t)(ra + w * 4096 + 512);
        }
        const u32x4 res = {cvt_pk_bf16(s0[0], s0[1]), cvt_pk_bf16(s0[2], s0[3]), cvt_pk_bf16(s1[0], s1[1]), cvt_pk_bf16(s1[2], s1[3])};
        gstore16_dev(out_, off, res);
        if (b == 0 && wave == 0) gstore8_dev(ws, WS_EPOCH, tagv);
    }
    }
}

extern "C" void kernel_launch(void* const* d_in, const int* in_sizes, int n_in, void* d_out, int out_size, void* d_ws, size_t ws_size,
                              hipStream_t stream) {
    (void)hipFuncSetAttribute((const void*)k_fused, hipFuncAttributeMaxDynamicSharedMemorySize, LDS_BYTES);
    k_fused<<<256, 512, LDS_BYTES, stream>>>(d_in[0], d_in[1], d_in[2], d_in[4], d_in[5], d_in[6], d_ws, d_out);
}
```
